```python
import torch

def _softmax(x, axis=-1):
    return torch.softmax(x, dim=axis)
def _gelu(x):
    return torch.nn.functional.gelu(x, approximate="tanh")
def _silu(x):
    return torch.nn.functional.silu(x)
def _sigmoid(x):
    return torch.sigmoid(x)
def _layernorm(x, axis=-1, eps=1e-5):
    m = x.mean(dim=axis, keepdim=True)
    v = x.var(dim=axis, keepdim=True, unbiased=False)
    return (x - m) / torch.sqrt(v + eps)
def _scatter_add(tgt, idx, src, axis=0):
    return tgt.clone().index_add_(axis, idx, src)
def _segment_sum(src, seg, n):
    out = torch.zeros((n,) + src.shape[1:], dtype=src.dtype)
    return out.index_add_(0, seg, src)
def _pad(x, pw):
    flat = tuple(v for pair in reversed(pw) for v in pair)
    return torch.nn.functional.pad(x, flat)

kernel_name = 'sequential_preLN__spectral_gfnet__glu_gated_glu__prompt'

def setup_inputs():

    _w = (lambda a: (lambda t: t.to(torch.bfloat16) if t.is_floating_point() else t)(torch.as_tensor(a)))
    def _A_setup():
        B, S, D = 1, 2048, 448
        Df = D // 2 + 1
        return {
            "x": _w((lambda *s: torch.randn(*s, dtype=torch.bfloat16) * 0.05)(B, S, D)),
            "w_r": _w((lambda *s: torch.randn(*s, dtype=torch.bfloat16) * 0.4)(S, Df) * 0.02),
            "w_i": _w((lambda *s: torch.randn(*s, dtype=torch.bfloat16) * 0.4)(S, Df) * 0.02),
        }
    _A_in = _A_setup()

    def _B_setup():
        B, S, D, F = 1, 2048, 448, 1792
        return {
            "x": _w((lambda *s: torch.randn(*s, dtype=torch.bfloat16) * 0.05)(B, S, D)),
            "w_gate": _w((lambda *s: torch.randn(*s, dtype=torch.bfloat16) * 0.05)(D, F)),
            "w_up": _w((lambda *s: torch.randn(*s, dtype=torch.bfloat16) * 0.05)(D, F)),
            "w_down": _w((lambda *s: torch.randn(*s, dtype=torch.bfloat16) * 0.05)(F, D)),
        }
    _B_in = _B_setup()

    return {
        "A_x": _A_in["x"],
        "A_w_r": _A_in["w_r"],
        "A_w_i": _A_in["w_i"],
        "B_x": _B_in["x"],
        "B_w_gate": _B_in["w_gate"],
        "B_w_up": _B_in["w_up"],
        "B_w_down": _B_in["w_down"],
    }

def reference(A_x, A_w_r, A_w_i, B_x, B_w_gate, B_w_up, B_w_down):
    def _A_ref(x, w_r, w_i):
        B, S, D = x.shape
        X = (lambda x, axes=(-2,-1): torch.fft.rfft2(x.float(), dim=axes))(x, axes=(-2, -1))
        W = w_r + 1j * w_i
        X = X * W
        y = (lambda x, s, axes=(-2,-1): torch.fft.irfft2(x, s=s, dim=axes).to(torch.bfloat16))(X, (S, D), axes=(-2, -1))
        return y

    def _B_ref(x, w_gate, w_up, w_down):
        g = torch.einsum("bsd,df->bsf", x, w_gate)
        u = torch.einsum("bsd,df->bsf", x, w_up)
        h = _sigmoid(g) * u
        return torch.einsum("bsf,fd->bsd", h, w_down)

    x = A_x
    h = x + _A_ref(_layernorm(x, axis=-1), A_w_r, A_w_i)
    h2 = h + _B_ref(_layernorm(h, axis=-1), B_w_gate, B_w_up, B_w_down)
    return h2
```

```cpp
#include <hip/hip_runtime.h>
#include <stdint.h>

typedef __attribute__((ext_vector_type(8))) __bf16 bf16x8;
typedef __attribute__((ext_vector_type(2))) __bf16 bf16x2;
typedef __attribute__((ext_vector_type(2))) float f32x2;
typedef __attribute__((ext_vector_type(4))) float f32x4;
typedef __attribute__((ext_vector_type(16))) float f32x16;
typedef __attribute__((ext_vector_type(4))) uint32_t u32x4;
typedef __attribute__((ext_vector_type(2))) uint32_t u32x2;
typedef __attribute__((ext_vector_type(4))) short s16x4;
#define LDSP __attribute__((address_space(3)))
#define DI __device__ __forceinline__

DI f32x4 mfma16(u32x4 a, u32x4 b, f32x4 c) {
  return __builtin_amdgcn_mfma_f32_16x16x32_bf16(__builtin_bit_cast(bf16x8, a), __builtin_bit_cast(bf16x8, b), c, 0, 0, 0);
}
DI f32x16 mfma32(u32x4 a, u32x4 b, f32x16 c) {
  return __builtin_amdgcn_mfma_f32_32x32x16_bf16(__builtin_bit_cast(bf16x8, a), __builtin_bit_cast(bf16x8, b), c, 0, 0, 0);
}
DI uint32_t pk(float a, float b) {
  f32x2 v = {a, b};
  return __builtin_bit_cast(uint32_t, __builtin_convertvector(v, bf16x2));
}
DI float lo16(uint32_t v) { return __builtin_bit_cast(float, v << 16); }
DI float hi16(uint32_t v) { return __builtin_bit_cast(float, v & 0xffff0000u); }
DI float cosr(float r) { return __builtin_amdgcn_cosf(r); }
DI float sinr(float r) { return __builtin_amdgcn_sinf(r); }
template <int N> DI float ror16(float v) {
  return __builtin_bit_cast(float, __builtin_amdgcn_update_dpp(0, __builtin_bit_cast(int, v), 0x120 + N, 0xf, 0xf, true));
}
DI float sum16(float v) {
  v += ror16<8>(v); v += ror16<4>(v); v += ror16<2>(v); v += ror16<1>(v);
  return v;
}

constexpr int S = 2048, D = 448, F = 1792;
constexpr size_t OFF_Z = 0;
constexpr size_t OFF_U = 2u << 20;
constexpr size_t OFF_H = 4u << 20;
constexpr size_t OFF_L = 6u << 20;
constexpr size_t OFF_WGU = 8u << 20;
constexpr size_t OFF_WD = 12u << 20;
constexpr size_t OFF_HH = 14u << 20;

DI void transpose8x8_store(const u32x4 (&in)[8], u32x4* dst) {
#pragma unroll
  for (int q = 0; q < 4; ++q) {
    u32x4 a, b;
#pragma unroll
    for (int jj = 0; jj < 4; ++jj) {
      a[jj] = __builtin_amdgcn_perm(in[2 * jj + 1][q], in[2 * jj][q], 0x05040100u);
      b[jj] = __builtin_amdgcn_perm(in[2 * jj + 1][q], in[2 * jj][q], 0x07060302u);
    }
    dst[2 * q] = a;
    dst[2 * q + 1] = b;
  }
}

__global__ __launch_bounds__(256) void k1(const uint16_t* __restrict__ x, const uint16_t* __restrict__ wg,
                                          const uint16_t* __restrict__ wu, const uint16_t* __restrict__ wd,
                                          char* __restrict__ ws) {
  constexpr int RS = 912;
  __shared__ __attribute__((aligned(16))) char img[16 * RS];
  const int t = threadIdx.x, lane = t & 63, w = t >> 6;
  if (blockIdx.x >= 128) {
    const int u = (blockIdx.x - 128) * 4 + w;
    u32x4 in[8];
    if (u < 392) {
      const int mat = u / 196, rem = u % 196, fg = rem / 28, ks = rem % 28;
      const int c = lane & 31, h = lane >> 5;
      const uint16_t* src = (mat ? wu : wg) + (size_t)(16 * ks + 8 * h) * F + 256 * fg + 8 * c;
#pragma unroll
      for (int j = 0; j < 8; ++j) in[j] = *(const u32x4*)(src + (size_t)j * F);
      const int f = 256 * fg + 8 * c, ft = f >> 4, row = (f & 15) + 16 * mat;
      u32x4* dst = (u32x4*)(ws + OFF_WGU) + ((ft * 28 + ks) * 64 + h * 32 + row);
      transpose8x8_store(in, dst);
    } else if (u < 588) {
      const int it = (u - 392) * 64 + lane;
      const int ch = it % 56, fh = it / 56, h = fh & 1, ft = fh >> 1;
      const uint16_t* src = wd + (size_t)(16 * ft + 4 * h) * D + 8 * ch;
#pragma unroll
      for (int j = 0; j < 8; ++j) in[j] = *(const u32x4*)(src + (size_t)(8 * (j >> 2) + (j & 3)) * D);
      const int n = 8 * ch;
      u32x4* dst = (u32x4*)(ws + OFF_WD) + (((n >> 5) * 112 + ft) * 64 + h * 32 + (n & 31));
      transpose8x8_store(in, dst);
    }
    return;
  }
  const int s2 = blockIdx.x;
  {
    const int s1 = t >> 4, c = t & 15;
    const u32x4* xr = (const u32x4*)(x + (size_t)(128 * s1 + s2) * D);
    u32x4 v[4];
    v[0] = xr[c]; v[1] = xr[c + 16]; v[2] = xr[c + 32];
    const bool ok3 = c < 8;
    v[3] = xr[ok3 ? c + 48 : c];
    float f[32];
    float sum = 0.f;
#pragma unroll
    for (int i = 0; i < 4; ++i)
#pragma unroll
      for (int q = 0; q < 4; ++q) { f[8 * i + 2 * q] = lo16(v[i][q]); f[8 * i + 2 * q + 1] = hi16(v[i][q]); }
    float s3 = 0.f;
#pragma unroll
    for (int e = 0; e < 24; ++e) sum += f[e];
#pragma unroll
    for (int e = 24; e < 32; ++e) s3 += f[e];
    sum += ok3 ? s3 : 0.f;
    const float mean = sum16(sum) * (1.f / D);
    float sq = 0.f, q3 = 0.f;
#pragma unroll
    for (int e = 0; e < 32; ++e) f[e] -= mean;
#pragma unroll
    for (int e = 0; e < 24; ++e) sq += f[e] * f[e];
#pragma unroll
    for (int e = 24; e < 32; ++e) q3 += f[e] * f[e];
    sq += ok3 ? q3 : 0.f;
    const float rstd = __builtin_amdgcn_rsqf(sum16(sq) * (1.f / D) + 1e-5f);
#pragma unroll
    for (int i = 0; i < 4; ++i) {
      u32x4 o;
#pragma unroll
      for (int q = 0; q < 4; ++q) o[q] = pk(f[8 * i + 2 * q] * rstd, f[8 * i + 2 * q + 1] * rstd);
      if (i < 3 || ok3) *(u32x4*)(img + s1 * RS + (c + 16 * i) * 16) = o;
    }
  }
  const int l15 = lane & 15, g = lane >> 4;
  u32x4 Ec, Es, Tr, Ti;
  {
    float cc[8], ss[8];
#pragma unroll
    for (int j = 0; j < 8; ++j) {
      const int d1 = 8 * g + j;
      const float a = (float)((d1 * l15) % 28) * (1.f / 28.f);
      const bool ok = d1 < 28;
      cc[j] = ok ? cosr(a) : 0.f;
      ss[j] = ok ? -sinr(a) : 0.f;
    }
#pragma unroll
    for (int q = 0; q < 4; ++q) { Ec[q] = pk(cc[2 * q], cc[2 * q + 1]); Es[q] = pk(ss[2 * q], ss[2 * q + 1]); }
    float c2[4], s2v[4];
#pragma unroll
    for (int j = 0; j < 4; ++j) {
      const int s = 128 * (4 * j + g) + s2;
      const float a = (float)((s * l15) & 2047) * (1.f / 2048.f);
      c2[j] = cosr(a); s2v[j] = sinr(a);
    }
    Tr[0] = pk(c2[0], c2[1]); Tr[1] = pk(c2[2], c2[3]); Tr[2] = pk(s2v[0], s2v[1]); Tr[3] = pk(s2v[2], s2v[3]);
    Ti[0] = Tr[2] ^ 0x80008000u; Ti[1] = Tr[3] ^ 0x80008000u; Ti[2] = Tr[0]; Ti[3] = Tr[1];
  }
  __syncthreads();
  f32x4 xr_[4], xi_[4];
  const f32x4 zero = {0.f, 0.f, 0.f, 0.f};
  {
    const int q = (lane >> 2) & 3, p = lane & 3;
    const int d1a = 8 * g + q, d1b = (g == 3) ? d1a : d1a + 4;
#pragma unroll
    for (int tt = 0; tt < 4; ++tt) {
      const char* base = img + (4 * tt + p) * RS + 8 * w;
      s16x4 a0 = __builtin_amdgcn_ds_read_tr16_b64_v4i16((LDSP s16x4*)(base + d1a * 32));
      s16x4 a1 = __builtin_amdgcn_ds_read_tr16_b64_v4i16((LDSP s16x4*)(base + d1b * 32));
      u32x2 b0 = __builtin_bit_cast(u32x2, a0), b1 = __builtin_bit_cast(u32x2, a1);
      u32x4 a = {b0[0], b0[1], b1[0], b1[1]};
      xr_[tt] = mfma16(a, Ec, zero);
      xi_[tt] = mfma16(a, Es, zero);
    }
  }
  f32x4 zr[4], zi[4];
#pragma unroll
  for (int r = 0; r < 4; ++r) {
    u32x4 b = {pk(xr_[0][r], xr_[1][r]), pk(xr_[2][r], xr_[3][r]), pk(xi_[0][r], xi_[1][r]), pk(xi_[2][r], xi_[3][r])};
    zr[r] = mfma16(Tr, b, zero);
    zi[r] = mfma16(Ti, b, zero);
  }
  if (l15 < 15) {
    uint32_t* Z = (uint32_t*)(ws + OFF_Z);
#pragma unroll
    for (int rr = 0; rr < 4; ++rr) {
      u32x4 o = {pk(zr[0][rr], zi[0][rr]), pk(zr[1][rr], zi[1][rr]), pk(zr[2][rr], zi[2][rr]), pk(zr[3][rr], zi[3][rr])};
      const int task = l15 * 16 + 4 * g + rr;
      *(u32x4*)(Z + (size_t)task * 2048 + s2 * 16 + 4 * w) = o;
    }
  }
}

__global__ __launch_bounds__(256) void k2(const uint16_t* __restrict__ wr, const uint16_t* __restrict__ wi,
                                          char* __restrict__ ws) {
  __shared__ u32x4 ybuf[4][2][64];
  const int t = threadIdx.x, lane = t & 63, w = t >> 6, l15 = lane & 15, g = lane >> 4;
  const int task = blockIdx.x, kd1 = task >> 4, k1 = task & 15;
  const uint32_t* Z = (const uint32_t*)(ws + OFF_Z) + (size_t)task * 2048;
  u32x4 zf[8];
#pragma unroll
  for (int i = 0; i < 8; ++i) zf[i] = *(const u32x4*)(Z + (16 * i + l15) * 16 + 4 * g);
  float fwr[8], fwi[8];
  {
    const int kd = kd1 + 28 * l15;
    const bool mir = kd > 224;
    const int col = mir ? 448 - kd : kd;
    const float sc = 1.f / (2048.f * 448.f);
    const float sci = mir ? -sc : sc;
#pragma unroll
    for (int e = 0; e < 8; ++e) {
      const int k2 = 32 * w + 16 * (e >> 2) + 4 * g + (e & 3);
      const int k = k1 + 16 * k2;
      const int row = mir ? ((2048 - k) & 2047) : k;
      fwr[e] = lo16(wr[row * 225 + col]) * sc;
      fwi[e] = lo16(wi[row * 225 + col]) * sci;
    }
  }
  u32x4 Br, Bi, Ar, Ai, C[2][4], Sn[2][4];
  {
    const int kd = kd1 + 28 * l15;
#pragma unroll
    for (int q = 0; q < 4; ++q) {
      const float a = (float)(((4 * g + q) * kd) % 448) * (1.f / 448.f);
      const float c = cosr(a), s = sinr(a);
      Br[q] = pk(c, s); Bi[q] = pk(-s, c);
    }
    float c4[4], s4[4];
#pragma unroll
    for (int q = 0; q < 4; ++q) {
      const float a = (float)((l15 * (kd1 + 28 * (4 * g + q))) % 448) * (1.f / 448.f);
      c4[q] = cosr(a); s4[q] = sinr(a);
    }
    Ar[0] = pk(c4[0], c4[1]); Ar[1] = pk(c4[2], c4[3]);
    Ai[0] = pk(s4[0], s4[1]); Ai[1] = pk(s4[2], s4[3]);
    Ar[2] = Ai[0] ^ 0x80008000u; Ar[3] = Ai[1] ^ 0x80008000u; Ai[2] = Ar[0]; Ai[3] = Ar[1];
#pragma unroll
    for (int tt = 0; tt < 2; ++tt)
#pragma unroll
      for (int ks = 0; ks < 4; ++ks) {
        const int a = 32 * w + 16 * tt + l15;
#pragma unroll
        for (int q = 0; q < 4; ++q) {
          const int b0 = 32 * ks + 16 * (q >> 1) + 4 * g + 2 * (q & 1);
          const float a0 = (float)((a * b0) & 127) * (1.f / 128.f), a1 = (float)((a * (b0 + 1)) & 127) * (1.f / 128.f);
          C[tt][ks][q] = pk(cosr(a0), cosr(a1));
          Sn[tt][ks][q] = pk(sinr(a0), sinr(a1));
        }
      }
  }
  const f32x4 zero = {0.f, 0.f, 0.f, 0.f};
  u32x4 Pr[4], Pi[4];
#pragma unroll
  for (int ks = 0; ks < 4; ++ks) {
    f32x4 r0 = mfma16(zf[2 * ks], Br, zero), i0 = mfma16(zf[2 * ks], Bi, zero);
    f32x4 r1 = mfma16(zf[2 * ks + 1], Br, zero), i1 = mfma16(zf[2 * ks + 1], Bi, zero);
    Pr[ks] = u32x4{pk(r0[0], r0[1]), pk(r0[2], r0[3]), pk(r1[0], r1[1]), pk(r1[2], r1[3])};
    Pi[ks] = u32x4{pk(i0[0], i0[1]), pk(i0[2], i0[3]), pk(i1[0], i1[1]), pk(i1[2], i1[3])};
  }
  f32x4 x2r[2] = {zero, zero}, x2i[2] = {zero, zero};
#pragma unroll
  for (int ks = 0; ks < 4; ++ks) {
    const u32x4 Nr = Pr[ks] ^ 0x80008000u;
#pragma unroll
    for (int tt = 0; tt < 2; ++tt) {
      x2r[tt] = mfma16(C[tt][ks], Pr[ks], x2r[tt]);
      x2i[tt] = mfma16(C[tt][ks], Pi[ks], x2i[tt]);
      x2r[tt] = mfma16(Sn[tt][ks], Pi[ks], x2r[tt]);
      x2i[tt] = mfma16(Sn[tt][ks], Nr, x2i[tt]);
    }
  }
  {
    float yr[8], yi[8];
#pragma unroll
    for (int e = 0; e < 8; ++e) {
      const float a = x2r[e >> 2][e & 3], b = x2i[e >> 2][e & 3];
      yr[e] = a * fwr[e] - b * fwi[e];
      yi[e] = a * fwi[e] + b * fwr[e];
    }
    ybuf[w][0][lane] = u32x4{pk(yr[0], yr[1]), pk(yr[2], yr[3]), pk(yr[4], yr[5]), pk(yr[6], yr[7])};
    ybuf[w][1][lane] = u32x4{pk(yi[0], yi[1]), pk(yi[2], yi[3]), pk(yi[4], yi[5]), pk(yi[6], yi[7])};
  }
  __syncthreads();
  f32x4 x3r[2] = {zero, zero}, x3i[2] = {zero, zero};
#pragma unroll
  for (int ks = 0; ks < 4; ++ks) {
    const u32x4 Yr = ybuf[ks][0][lane], Yi = ybuf[ks][1][lane];
    const u32x4 Ni = Yi ^ 0x80008000u;
#pragma unroll
    for (int tt = 0; tt < 2; ++tt) {
      x3r[tt] = mfma16(Yr, C[tt][ks], x3r[tt]);
      x3i[tt] = mfma16(Yr, Sn[tt][ks], x3i[tt]);
      x3r[tt] = mfma16(Ni, Sn[tt][ks], x3r[tt]);
      x3i[tt] = mfma16(Yi, C[tt][ks], x3i[tt]);
    }
  }
  uint32_t* U = (uint32_t*)(ws + OFF_U);
  const int slot = ((((kd1 >> 2) * 4 + (k1 & 3)) * 4 + (k1 >> 2)) * 4 + (kd1 & 3)) * 16 + 4 * g;
#pragma unroll
  for (int tt = 0; tt < 2; ++tt) {
    u32x4 b = {pk(x3r[tt][0], x3r[tt][1]), pk(x3r[tt][2], x3r[tt][3]), pk(x3i[tt][0], x3i[tt][1]), pk(x3i[tt][2], x3i[tt][3])};
    f32x4 ur = mfma16(Ar, b, zero), ui = mfma16(Ai, b, zero);
    u32x4 o = {pk(ur[0], ui[0]), pk(ur[1], ui[1]), pk(ur[2], ui[2]), pk(ur[3], ui[3])};
    const int s2 = 32 * w + 16 * tt + l15;
    *(u32x4*)(U + (size_t)s2 * 4096 + slot) = o;
  }
}

__global__ __launch_bounds__(256) void k3(const uint16_t* __restrict__ x, char* __restrict__ ws) {
  constexpr int RS = 912;
  __shared__ __attribute__((aligned(16))) char img[16 * RS];
  const int t = threadIdx.x, lane = t & 63, w = t >> 6, l15 = lane & 15, g = lane >> 4;
  const int s2 = blockIdx.x;
  const uint32_t* U = (const uint32_t*)(ws + OFF_U) + (size_t)s2 * 4096;
  u32x4 uf[4];
  {
    const int gp = l15 >> 2, r = l15 & 3;
#pragma unroll
    for (int tt = 0; tt < 4; ++tt)
#pragma unroll
      for (int i = 0; i < 4; ++i) {
        const int gq = (tt == 3 && gp == 3) ? 0 : gp;
        uf[tt][i] = U[(((tt * 4 + i) * 4 + g) * 4 + gq) * 16 + 4 * w + r];
      }
  }
  const int s1r = t >> 4, c = t & 15;
  const bool ok3 = c < 8;
  u32x4 xv[4];
  {
    const u32x4* xr = (const u32x4*)(x + (size_t)(128 * s1r + s2) * D);
    xv[0] = xr[c]; xv[1] = xr[c + 16]; xv[2] = xr[c + 32]; xv[3] = xr[ok3 ? c + 48 : c];
  }
  u32x4 Br, Bi, E0, E1;
  {
    const int s = 128 * l15 + s2;
#pragma unroll
    for (int i = 0; i < 4; ++i) {
      const float a = (float)((s * (4 * g + i)) & 2047) * (1.f / 2048.f);
      const float cc = cosr(a), ss = sinr(a);
      Br[i] = pk(cc, -ss); Bi[i] = pk(ss, cc);
    }
    float c0[4], s0[4], c1[4], s1v[4];
#pragma unroll
    for (int j = 0; j < 4; ++j) {
      const int kd1 = 4 * j + g;
      const float wgt = (kd1 == 15) ? 0.f : ((kd1 == 0 || kd1 == 14) ? 1.f : 2.f);
      const float a0 = (float)((l15 * kd1) % 28) * (1.f / 28.f), a1 = (float)(((l15 + 16) * kd1) % 28) * (1.f / 28.f);
      c0[j] = wgt * cosr(a0); s0[j] = -wgt * sinr(a0);
      c1[j] = wgt * cosr(a1); s1v[j] = -wgt * sinr(a1);
      if (kd1 == 0 || kd1 == 14) { s0[j] = 0.f; s1v[j] = 0.f; }
    }
    E0 = u32x4{pk(c0[0], c0[1]), pk(c0[2], c0[3]), pk(s0[0], s0[1]), pk(s0[2], s0[3])};
    E1 = u32x4{pk(c1[0], c1[1]), pk(c1[2], c1[3]), pk(s1v[0], s1v[1]), pk(s1v[2], s1v[3])};
  }
  const f32x4 zero = {0.f, 0.f, 0.f, 0.f};
  f32x4 vr[4], vi[4];
#pragma unroll
  for (int tt = 0; tt < 4; ++tt) { vr[tt] = mfma16(uf[tt], Br, zero); vi[tt] = mfma16(uf[tt], Bi, zero); }
  f32x4 y0[4], y1[4];
#pragma unroll
  for (int r = 0; r < 4; ++r) {
    u32x4 a = {pk(vr[0][r], vr[1][r]), pk(vr[2][r], vr[3][r]), pk(vi[0][r], vi[1][r]), pk(vi[2][r], vi[3][r])};
    y0[r] = mfma16(a, E0, zero);
    y1[r] = mfma16(a, E1, zero);
  }
#pragma unroll
  for (int rr = 0; rr < 4; ++rr) {
    char* p = img + (4 * g + rr) * RS + l15 * 32 + 8 * w;
    *(u32x2*)p = u32x2{pk(y0[0][rr], y0[1][rr]), pk(y0[2][rr], y0[3][rr])};
    if (l15 < 12) *(u32x2*)(p + 512) = u32x2{pk(y1[0][rr], y1[1][rr]), pk(y1[2][rr], y1[3][rr])};
  }
  __syncthreads();
  float f[32];
  u32x4 hb[4];
#pragma unroll
  for (int i = 0; i < 4; ++i) {
    const u32x4 yv = *(const u32x4*)(img + s1r * RS + ((i < 3 || ok3) ? c + 16 * i : c) * 16);
#pragma unroll
    for (int q = 0; q < 4; ++q) {
      hb[i][q] = pk(lo16(xv[i][q]) + lo16(yv[q]), hi16(xv[i][q]) + hi16(yv[q]));
      f[8 * i + 2 * q] = lo16(hb[i][q]); f[8 * i + 2 * q + 1] = hi16(hb[i][q]);
    }
  }
  const int m = 16 * s2 + s1r;
  {
    u32x4* H = (u32x4*)(ws + OFF_H) + (size_t)m * 56;
    H[c] = hb[0]; H[c + 16] = hb[1]; H[c + 32] = hb[2];
    if (ok3) H[c + 48] = hb[3];
  }
  float sum = 0.f, s3 = 0.f;
#pragma unroll
  for (int e = 0; e < 24; ++e) sum += f[e];
#pragma unroll
  for (int e = 24; e < 32; ++e) s3 += f[e];
  sum += ok3 ? s3 : 0.f;
  const float mean = sum16(sum) * (1.f / D);
  float sq = 0.f, q3 = 0.f;
#pragma unroll
  for (int e = 0; e < 32; ++e) f[e] -= mean;
#pragma unroll
  for (int e = 0; e < 24; ++e) sq += f[e] * f[e];
#pragma unroll
  for (int e = 24; e < 32; ++e) q3 += f[e] * f[e];
  sq += ok3 ? q3 : 0.f;
  const float rstd = __builtin_amdgcn_rsqf(sum16(sq) * (1.f / D) + 1e-5f);
  char* L = ws + OFF_L + (size_t)(m >> 3) * 7168 + (m & 7) * 128;
#pragma unroll
  for (int i = 0; i < 4; ++i) {
    u32x4 o;
#pragma unroll
    for (int q = 0; q < 4; ++q) o[q] = pk(f[8 * i + 2 * q] * rstd, f[8 * i + 2 * q + 1] * rstd);
    const int ch = c + 16 * i;
    if (i < 3 || ok3) *(u32x4*)(L + (ch >> 3) * 1024 + (((ch & 7) ^ ((m >> 1) & 7)) << 4)) = o;
  }
}

#define GLOAD(dst, ptr) asm volatile("global_load_dwordx4 %0, %1, off" : "=v"(dst) : "v"(ptr) : "memory")
__global__ __launch_bounds__(512, 2) void k4(char* __restrict__ ws) {
  __shared__ __attribute__((aligned(16))) char At[112 * 1024];
  const int t = threadIdx.x, lane = t & 63;
  const int w = __builtin_amdgcn_readfirstlane(t >> 6);
  const int mt = blockIdx.x / 14, nt = blockIdx.x % 14;
  const int ft = nt * 8 + w;
  const char* Lg = ws + OFF_L + (size_t)mt * (112 * 1024) + (size_t)(2 * w) * 7168 + lane * 16;
  const char* Wg = ws + OFF_WGU + (size_t)ft * (28 * 1024) + lane * 16;
  u32x4 wf[28];
#pragma unroll
  for (int seg = 0; seg < 7; ++seg) {
    __builtin_amdgcn_global_load_lds((const uint32_t*)(Lg + seg * 1024), (LDSP uint32_t*)(At + (2 * w) * 7168 + seg * 1024), 16, 0, 0);
    __builtin_amdgcn_global_load_lds((const uint32_t*)(Lg + 7168 + seg * 1024), (LDSP uint32_t*)(At + (2 * w + 1) * 7168 + seg * 1024), 16, 0, 0);
#pragma unroll
    for (int i = 0; i < 4; ++i) GLOAD(wf[4 * seg + i], Wg + (4 * seg + i) * 1024);
  }
  f32x16 acc[4];
#pragma unroll
  for (int q = 0; q < 4; ++q)
#pragma unroll
    for (int e = 0; e < 16; ++e) acc[q][e] = 0.f;
  const int ml = lane & 31, h = lane >> 5;
  const char* abase = At + (ml >> 3) * 7168 + (ml & 7) * 128;
  const int sw = (ml >> 1) & 7;
#define SEG(seg, N)                                                                                     \
  {                                                                                                     \
    asm volatile("s_waitcnt vmcnt(" #N ")" : "+v"(wf[4 * seg]), "+v"(wf[4 * seg + 1]), "+v"(wf[4 * seg + 2]), "+v"(wf[4 * seg + 3]) :: "memory"); \
    __builtin_amdgcn_s_barrier();                                                                       \
    _Pragma("unroll") for (int i = 0; i < 4; ++i) {                                                     \
      _Pragma("unroll") for (int q = 0; q < 4; ++q) {                                                   \
        const u32x4 b = *(const u32x4*)(abase + q * 28672 + seg * 1024 + (((2 * i + h) ^ sw) << 4));    \
        acc[q] = mfma32(wf[4 * seg + i], b, acc[q]);                                                    \
      }                                                                                                 \
    }                                                                                                   \
  }
  SEG(0, 36) SEG(1, 30) SEG(2, 24) SEG(3, 18) SEG(4, 12) SEG(5, 6) SEG(6, 0)
#undef SEG
  u32x4* HH = (u32x4*)(ws + OFF_HH);
#pragma unroll
  for (int q = 0; q < 4; ++q) {
    float o[8];
#pragma unroll
    for (int e = 0; e < 8; ++e) {
      const float sg = __builtin_amdgcn_rcpf(1.f + __builtin_amdgcn_exp2f(acc[q][e] * -1.44269504088896f));
      o[e] = sg * acc[q][8 + e];
    }
    HH[((size_t)(mt * 4 + q) * 112 + ft) * 64 + lane] = u32x4{pk(o[0], o[1]), pk(o[2], o[3]), pk(o[4], o[5]), pk(o[6], o[7])};
  }
}

__global__ __launch_bounds__(256, 1) void k5(char* __restrict__ ws, uint16_t* __restrict__ out) {
  constexpr int RS = 272;
  __shared__ __attribute__((aligned(16))) char red[4 * 64 * RS];
  const int t = threadIdx.x, lane = t & 63;
  const int w = __builtin_amdgcn_readfirstlane(t >> 6);
  const int mt = blockIdx.x / 7, nt = blockIdx.x % 7;
  const u32x4* HH = (const u32x4*)(ws + OFF_HH) + ((size_t)(2 * mt) * 112 + 28 * w) * 64 + lane;
  const u32x4* WD = (const u32x4*)(ws + OFF_WD) + ((size_t)(2 * nt) * 112 + 28 * w) * 64 + lane;
  const int em = t >> 2, en = (t & 3) * 16;
  const int gm = 64 * mt + em;
  const u32x4* Hp = (const u32x4*)(ws + OFF_H + (size_t)gm * 896 + (64 * nt + en) * 2);
  f32x16 acc[2][2];
#pragma unroll
  for (int a = 0; a < 2; ++a)
#pragma unroll
    for (int b = 0; b < 2; ++b)
#pragma unroll
      for (int e = 0; e < 16; ++e) acc[a][b][e] = 0.f;
  constexpr int PF = 14;
  u32x4 fa[PF][2], fb[PF][2];
#pragma unroll
  for (int i = 0; i < PF; ++i) {
    fa[i][0] = WD[i * 64]; fa[i][1] = WD[(112 + i) * 64];
    fb[i][0] = HH[i * 64]; fb[i][1] = HH[(112 + i) * 64];
  }
  const u32x4 h0 = Hp[0], h1 = Hp[1];
#pragma unroll
  for (int i = 0; i < 28; ++i) {
    const int s = i % PF;
#pragma unroll
    for (int a = 0; a < 2; ++a)
#pragma unroll
      for (int b = 0; b < 2; ++b) acc[a][b] = mfma32(fa[s][a], fb[s][b], acc[a][b]);
    if (i + PF < 28) {
      fa[s][0] = WD[(i + PF) * 64]; fa[s][1] = WD[(112 + i + PF) * 64];
      fb[s][0] = HH[(i + PF) * 64]; fb[s][1] = HH[(112 + i + PF) * 64];
    }
  }
  {
    const int ml = lane & 31, h = lane >> 5;
#pragma unroll
    for (int a = 0; a < 2; ++a)
#pragma unroll
      for (int b = 0; b < 2; ++b)
#pragma unroll
        for (int q = 0; q < 4; ++q) {
          f32x4 v = {acc[a][b][4 * q], acc[a][b][4 * q + 1], acc[a][b][4 * q + 2], acc[a][b][4 * q + 3]};
          *(f32x4*)(red + (w * 64 + 32 * b + ml) * RS + (32 * a + 8 * q + 4 * h) * 4) = v;
        }
  }
  __syncthreads();
  f32x4 s[4];
#pragma unroll
  for (int q = 0; q < 4; ++q) {
    s[q] = *(const f32x4*)(red + em * RS + (en + 4 * q) * 4);
#pragma unroll
    for (int ww = 1; ww < 4; ++ww) s[q] += *(const f32x4*)(red + (ww * 64 + em) * RS + (en + 4 * q) * 4);
  }
  u32x4 o0, o1;
  o0[0] = pk(s[0][0] + lo16(h0[0]), s[0][1] + hi16(h0[0])); o0[1] = pk(s[0][2] + lo16(h0[1]), s[0][3] + hi16(h0[1]));
  o0[2] = pk(s[1][0] + lo16(h0[2]), s[1][1] + hi16(h0[2])); o0[3] = pk(s[1][2] + lo16(h0[3]), s[1][3] + hi16(h0[3]));
  o1[0] = pk(s[2][0] + lo16(h1[0]), s[2][1] + hi16(h1[0])); o1[1] = pk(s[2][2] + lo16(h1[1]), s[2][3] + hi16(h1[1]));
  o1[2] = pk(s[3][0] + lo16(h1[2]), s[3][1] + hi16(h1[2])); o1[3] = pk(s[3][2] + lo16(h1[3]), s[3][3] + hi16(h1[3]));
  const int srow = 128 * (gm & 15) + (gm >> 4);
  u32x4* op = (u32x4*)(out + (size_t)srow * D + 64 * nt + en);
  op[0] = o0; op[1] = o1;
}

extern "C" void kernel_launch(void* const* d_in, const int* in_sizes, int n_in, void* d_out, int out_size,
                              void* d_ws, size_t ws_size, hipStream_t stream) {
  const uint16_t* x = (const uint16_t*)d_in[0];
  const uint16_t* wr = (const uint16_t*)d_in[1];
  const uint16_t* wi = (const uint16_t*)d_in[2];
  const uint16_t* wg = (const uint16_t*)d_in[4];
  const uint16_t* wu = (const uint16_t*)d_in[5];
  const uint16_t* wd = (const uint16_t*)d_in[6];
  char* ws = (char*)d_ws;
  k1<<<128 + 147, 256, 0, stream>>>(x, wg, wu, wd, ws);
  k2<<<240, 256, 0, stream>>>(wr, wi, ws);
  k3<<<128, 256, 0, stream>>>(x, ws);
  k4<<<224, 512, 0, stream>>>(ws);
  k5<<<224, 256, 0, stream>>>(ws, (uint16_t*)d_out);
}
```

```cpp
#include <hip/hip_runtime.h>
#include <stdint.h>

typedef __attribute__((ext_vector_type(8))) __bf16 bf16x8;
typedef __attribute__((ext_vector_type(2))) __bf16 bf16x2;
typedef __attribute__((ext_vector_type(2))) float f32x2;
typedef __attribute__((ext_vector_type(4))) float f32x4;
typedef __attribute__((ext_vector_type(16))) float f32x16;
typedef __attribute__((ext_vector_type(4))) uint32_t u32x4;
typedef __attribute__((ext_vector_type(2))) uint32_t u32x2;
typedef __attribute__((ext_vector_type(4))) short s16x4;
typedef __amdgpu_buffer_rsrc_t rsrc_t;
#define LDSP __attribute__((address_space(3)))
#define DI __device__ __forceinline__
#define SCHED() __builtin_amdgcn_sched_barrier(0)

#define AUX_X 0
#define AUX_L 0

#define ST(i)
#define ST_END(ws, kid)
#define ST_BEGIN

DI f32x4 mfma16(u32x4 a, u32x4 b, f32x4 c) {
  return __builtin_amdgcn_mfma_f32_16x16x32_bf16(__builtin_bit_cast(bf16x8, a), __builtin_bit_cast(bf16x8, b), c, 0, 0, 0);
}
DI f32x16 mfma32(u32x4 a, u32x4 b, f32x16 c) {
  return __builtin_amdgcn_mfma_f32_32x32x16_bf16(__builtin_bit_cast(bf16x8, a), __builtin_bit_cast(bf16x8, b), c, 0, 0, 0);
}
DI uint32_t pk(float a, float b) {
  f32x2 v = {a, b};
  return __builtin_bit_cast(uint32_t, __builtin_convertvector(v, bf16x2));
}
DI float lo16(uint32_t v) { return __builtin_bit_cast(float, v << 16); }
DI float hi16(uint32_t v) { return __builtin_bit_cast(float, v & 0xffff0000u); }
DI float cosr(float r) { return __builtin_amdgcn_cosf(r); }
DI float sinr(float r) { return __builtin_amdgcn_sinf(r); }
template <int N> DI float ror16(float v) {
  return __builtin_bit_cast(float, __builtin_amdgcn_update_dpp(0, __builtin_bit_cast(int, v), 0x120 + N, 0xf, 0xf, true));
}
DI float sum32(float v) {
  v += ror16<8>(v); v += ror16<4>(v); v += ror16<2>(v); v += ror16<1>(v);
  const uint32_t u = __builtin_bit_cast(uint32_t, v);
  auto s = __builtin_amdgcn_permlane16_swap(u, u, false, false);
  return __builtin_bit_cast(float, (uint32_t)s[0]) + __builtin_bit_cast(float, (uint32_t)s[1]);
}
DI rsrc_t mk(const void* p, uint32_t n) { return __builtin_amdgcn_make_buffer_rsrc((void*)p, 0, n, 0x00020000); }
DI u32x4 bld(rsrc_t r, uint32_t voff, uint32_t soff) { return __builtin_amdgcn_raw_buffer_load_b128(r, voff, soff, 0); }
#define BST(v, r, voff, soff, aux) __builtin_amdgcn_raw_buffer_store_b128(v, r, voff, soff, aux)
#define BLDS(r, ldsptr, voff, soff) __builtin_amdgcn_raw_ptr_buffer_load_lds(r, (LDSP void*)(ldsptr), 16, voff, soff, 0, 0)
DI u32x4 trpair(const char* p0, const char* p1) {
  s16x4 a0 = __builtin_amdgcn_ds_read_tr16_b64_v4i16((LDSP s16x4*)p0);
  s16x4 a1 = __builtin_amdgcn_ds_read_tr16_b64_v4i16((LDSP s16x4*)p1);
  u32x2 b0 = __builtin_bit_cast(u32x2, a0), b1 = __builtin_bit_cast(u32x2, a1);
  return u32x4{b0[0], b0[1], b1[0], b1[1]};
}

constexpr int D = 448, F = 1792;
constexpr uint32_t OFF_Z = 0;
constexpr uint32_t OFF_U = 2u << 20;
constexpr uint32_t OFF_H = 4u << 20;
constexpr uint32_t OFF_L = 6u << 20;
constexpr uint32_t OFF_WGU = 8u << 20;
constexpr uint32_t OFF_WD = 12u << 20;
constexpr uint32_t OFF_HH = 14u << 20;
constexpr uint32_t OFF_WF = 22u << 20;
constexpr uint32_t WS_BYTES = 24u << 20;
constexpr uint32_t NEG2 = 0x80008000u;

DI void ln_row(float (&f)[16], bool ok1) {
  float s0 = 0.f, s1 = 0.f;
#pragma unroll
  for (int e = 0; e < 8; ++e) { s0 += f[e]; s1 += f[8 + e]; }
  const float mean = sum32(s0 + (ok1 ? s1 : 0.f)) * (1.f / D);
  float q0 = 0.f, q1 = 0.f;
#pragma unroll
  for (int e = 0; e < 16; ++e) f[e] -= mean;
#pragma unroll
  for (int e = 0; e < 8; ++e) { q0 += f[e] * f[e]; q1 += f[8 + e] * f[8 + e]; }
  const float rstd = __builtin_amdgcn_rsqf(sum32(q0 + (ok1 ? q1 : 0.f)) * (1.f / D) + 1e-5f);
#pragma unroll
  for (int e = 0; e < 16; ++e) f[e] *= rstd;
}

__global__ __launch_bounds__(512, 2) void k1(const uint16_t* __restrict__ x, const uint16_t* __restrict__ wr,
                                             const uint16_t* __restrict__ wi, char* __restrict__ ws) {
  constexpr int RS = 912;
  __shared__ __attribute__((aligned(16))) char lds[32768];
  ST_BEGIN;
  const int t = threadIdx.x, lane = t & 63, l15 = lane & 15, g = lane >> 4;
  const int w = __builtin_amdgcn_readfirstlane(t >> 6);
  const rsrc_t rws = mk(ws, WS_BYTES);
  if (blockIdx.x >= 128) {
    const int u = blockIdx.x - 128, k1 = u >> 3, rho = (u >> 1) & 3, tt = u & 1;
    const rsrc_t rr = mk(wr, 2048 * 225 * 2), ri = mk(wi, 2048 * 225 * 2);
    {
      const int i = 2 * w + (lane >> 5);
      const int k = k1 + 16 * (4 * (16 * tt + i) + rho);
      const uint32_t o0 = ((uint32_t)(k * 450) & ~15u) + (lane & 31) * 16;
      const uint32_t o1 = ((uint32_t)(((2048 - k) & 2047) * 450) & ~15u) + (lane & 31) * 16;
      BLDS(rr, lds + w * 1024, o0, 0);
      BLDS(rr, lds + 8192 + w * 1024, o1, 0);
      BLDS(ri, lds + 16384 + w * 1024, o0, 0);
      BLDS(ri, lds + 24576 + w * 1024, o1, 0);
    }
    const int a0 = (k1 * 450) & 15, a1 = (16 - a0) & 15;
    __syncthreads();
#pragma unroll
    for (int n = 0; n < 2; ++n) {
      const int kd1 = w + 8 * n;
      if (kd1 < 15) {
        const int kd = kd1 + 28 * l15;
        const bool mir = kd > 224;
        const char* p = lds + (mir ? 8192 + a1 + 2 * (448 - kd) : a0 + 2 * kd) + g * 2048;
        const uint32_t sg = mir ? 0x80000000u : 0u;
        u32x4 o;
#pragma unroll
        for (int r = 0; r < 4; ++r) {
          const uint32_t re = *(const uint16_t*)(p + r * 512), im = *(const uint16_t*)(p + 16384 + r * 512);
          o[r] = (re | (im << 16)) ^ sg;
        }
        BST(o, rws, lane * 16, OFF_WF + (((k1 * 15 + kd1) * 4 + rho) * 2 + tt) * 1024, AUX_X);
      }
    }
    ST_END(ws, 1);
    return;
  }
  const int s2 = blockIdx.x;
  const int s1 = t >> 5, c = t & 31;
  const bool ok1 = c < 24;
  const rsrc_t rx = mk(x, 2048 * 896);
  const u32x4 v0 = bld(rx, s1 * (128 * 896) + c * 16, s2 * 896);
  const u32x4 v1 = bld(rx, s1 * (128 * 896) + (ok1 ? c + 32 : c) * 16, s2 * 896);
  SCHED();
  u32x4 Ec, Es, Tr, Ti;
  if (w < 4) {
    const float kf = (float)l15;
    float cc[8], ss[8];
#pragma unroll
    for (int j = 0; j < 8; ++j) {
      const float a = (float)(8 * g + j) * (kf * (1.f / 28.f));
      cc[j] = cosr(a); ss[j] = sinr(-a);
    }
#pragma unroll
    for (int q = 0; q < 4; ++q) { Ec[q] = pk(cc[2 * q], cc[2 * q + 1]); Es[q] = pk(ss[2 * q], ss[2 * q + 1]); }
    if (g == 3) { Ec[2] = 0; Ec[3] = 0; Es[2] = 0; Es[3] = 0; }
    float c2[4], s2v[4];
#pragma unroll
    for (int j = 0; j < 4; ++j) {
      const float a = (float)(128 * (4 * j + g) + s2) * (kf * (1.f / 2048.f));
      c2[j] = cosr(a); s2v[j] = sinr(a);
    }
    Tr[0] = pk(c2[0], c2[1]); Tr[1] = pk(c2[2], c2[3]); Tr[2] = pk(s2v[0], s2v[1]); Tr[3] = pk(s2v[2], s2v[3]);
    Ti[0] = Tr[2] ^ NEG2; Ti[1] = Tr[3] ^ NEG2; Ti[2] = Tr[0]; Ti[3] = Tr[1];
  }
  SCHED();
  {
    float f[16];
#pragma unroll
    for (int q = 0; q < 4; ++q) {
      f[2 * q] = lo16(v0[q]); f[2 * q + 1] = hi16(v0[q]);
      f[8 + 2 * q] = lo16(v1[q]); f[8 + 2 * q + 1] = hi16(v1[q]);
    }
    ln_row(f, ok1);
    *(u32x4*)(lds + s1 * RS + c * 16) = u32x4{pk(f[0], f[1]), pk(f[2], f[3]), pk(f[4], f[5]), pk(f[6], f[7])};
    if (ok1) *(u32x4*)(lds + s1 * RS + c * 16 + 512) = u32x4{pk(f[8], f[9]), pk(f[10], f[11]), pk(f[12], f[13]), pk(f[14], f[15])};
  }
  ST(1);
  __syncthreads();
  ST(2);
  if (w < 4) {
    f32x4 xr_[4], xi_[4];
    const f32x4 zero = {0.f, 0.f, 0.f, 0.f};
    const int q = (lane >> 2) & 3, p = lane & 3;
    const char* b0 = lds + p * RS + 8 * w + (8 * g + q) * 32;
    const char* b1 = b0 + ((g == 3) ? 0 : 128);
#pragma unroll
    for (int tt = 0; tt < 4; ++tt) {
      const u32x4 a = trpair(b0 + tt * 4 * RS, b1 + tt * 4 * RS);
      xr_[tt] = mfma16(a, Ec, zero);
      xi_[tt] = mfma16(a, Es, zero);
    }
    f32x4 zr[4], zi[4];
#pragma unroll
    for (int r = 0; r < 4; ++r) {
      u32x4 b = {pk(xr_[0][r], xr_[1][r]), pk(xr_[2][r], xr_[3][r]), pk(xi_[0][r], xi_[1][r]), pk(xi_[2][r], xi_[3][r])};
      zr[r] = mfma16(Tr, b, zero);
      zi[r] = mfma16(Ti, b, zero);
    }
#pragma unroll
    for (int rr = 0; rr < 4; ++rr) {
      u32x4 o = {pk(zr[0][rr], zi[0][rr]), pk(zr[1][rr], zi[1][rr]), pk(zr[2][rr], zi[2][rr]), pk(zr[3][rr], zi[3][rr])};
      BST(o, rws, lane * 64, OFF_Z + s2 * 16384 + rr * 4096 + w * 16, AUX_X);
    }
  }
  ST_END(ws, 1);
}

template <int N, bool CONST_IS_B> DI void rot_acc(f32x4& tr, f32x4& ti, u32x4 d, u32x4 P, u32x4 Q, u32x4 nP, u32x4 nQ) {
  const u32x4 cr = (N == 0) ? P : (N == 1) ? Q : (N == 2) ? nP : nQ;
  const u32x4 ci = (N == 0) ? Q : (N == 1) ? nP : (N == 2) ? nQ : P;
  if (CONST_IS_B) { tr = mfma16(d, cr, tr); ti = mfma16(d, ci, ti); }
  else { tr = mfma16(cr, d, tr); ti = mfma16(ci, d, ti); }
}
template <int RHO> DI void k2_fwd(f32x4 (&tr)[2], f32x4 (&ti)[2], const u32x4 (&zf)[8], u32x4 P, u32x4 Q) {
  const u32x4 nP = P ^ NEG2, nQ = Q ^ NEG2;
#pragma unroll
  for (int tb = 0; tb < 2; ++tb) rot_acc<0, true>(tr[tb], ti[tb], zf[tb], P, Q, nP, nQ);
#pragma unroll
  for (int tb = 0; tb < 2; ++tb) rot_acc<(RHO) & 3, true>(tr[tb], ti[tb], zf[2 + tb], P, Q, nP, nQ);
#pragma unroll
  for (int tb = 0; tb < 2; ++tb) rot_acc<(2 * RHO) & 3, true>(tr[tb], ti[tb], zf[4 + tb], P, Q, nP, nQ);
#pragma unroll
  for (int tb = 0; tb < 2; ++tb) rot_acc<(3 * RHO) & 3, true>(tr[tb], ti[tb], zf[6 + tb], P, Q, nP, nQ);
}
template <int KS> DI void k2_inv(f32x4 (&ur)[2], f32x4 (&ui)[2], const u32x4 (&rf)[8], u32x4 P, u32x4 Q) {
  const u32x4 nP = P ^ NEG2, nQ = Q ^ NEG2;
#pragma unroll
  for (int tb = 0; tb < 2; ++tb) rot_acc<0, false>(ur[tb], ui[tb], rf[tb], P, Q, nP, nQ);
#pragma unroll
  for (int tb = 0; tb < 2; ++tb) rot_acc<(4 - (KS & 3)) & 3, false>(ur[tb], ui[tb], rf[2 + tb], P, Q, nP, nQ);
#pragma unroll
  for (int tb = 0; tb < 2; ++tb) rot_acc<(4 - ((2 * KS) & 3)) & 3, false>(ur[tb], ui[tb], rf[4 + tb], P, Q, nP, nQ);
#pragma unroll
  for (int tb = 0; tb < 2; ++tb) rot_acc<(4 - ((3 * KS) & 3)) & 3, false>(ur[tb], ui[tb], rf[6 + tb], P, Q, nP, nQ);
}

__global__ __launch_bounds__(256, 2) void k2(const uint16_t* __restrict__ wg, const uint16_t* __restrict__ wu,
                                             char* __restrict__ ws) {
  __shared__ __attribute__((aligned(16))) char lds[16384];
  ST_BEGIN;
  const int t = threadIdx.x, lane = t & 63, l15 = lane & 15, g = lane >> 4;
  const int w = __builtin_amdgcn_readfirstlane(t >> 6);
  const rsrc_t rws = mk(ws, WS_BYTES);
  if (blockIdx.x >= 240) {
    const int u = blockIdx.x - 240, nt = u / 14, kp = u % 14;
    const rsrc_t rg = mk(wg, D * F * 2), ru = mk(wu, D * F * 2);
    const uint32_t vo = (8 * w + g) * (F * 2) + l15 * 16, so = kp * 32 * (F * 2) + nt * 256;
    BLDS(rg, lds + w * 2048, vo, so);
    BLDS(rg, lds + w * 2048 + 1024, vo, so + 4 * F * 2);
    BLDS(ru, lds + 8192 + w * 2048, vo, so);
    BLDS(ru, lds + 8192 + w * 2048 + 1024, vo, so + 4 * F * 2);
    __syncthreads();
    const int q = (lane >> 2) & 3, p = lane & 3;
    const char* b = lds + (g & 1) * 8192 + (8 * (g >> 1) + q) * 256 + p * 8 + w * 64;
#pragma unroll
    for (int i = 0; i < 2; ++i)
#pragma unroll
      for (int ks = 0; ks < 2; ++ks) {
        const u32x4 o = trpair(b + ks * 4096 + i * 32, b + ks * 4096 + i * 32 + 1024);
        BST(o, rws, lane * 16, OFF_WGU + ((nt * 8 + 2 * w + i) * 28 + 2 * kp + ks) * 1024, AUX_X);
      }
    ST_END(ws, 2);
    return;
  }
  const int xcd = blockIdx.x & 7, j = blockIdx.x >> 3;
  const int k1 = 2 * xcd + (j >= 15), kd1 = j - (j >= 15 ? 15 : 0);
  u32x4 zf[8], wf[2];
  {
    const uint32_t so = OFF_Z + ((k1 & 3) * 64 + (k1 >> 2) * 16 + kd1) * 64;
#pragma unroll
    for (int i = 0; i < 8; ++i) zf[i] = bld(rws, l15 * 16384 + g * 16, so + i * (16 * 16384));
    wf[0] = bld(rws, lane * 16, OFF_WF + ((k1 * 15 + kd1) * 4 + w) * 2048);
    wf[1] = bld(rws, lane * 16, OFF_WF + ((k1 * 15 + kd1) * 4 + w) * 2048 + 1024);
  }
  SCHED();
  u32x4 Br, Bi, Ar, Ai, C1[2], S1[2], C2[2], S2[2];
  {
    const float lf = (float)l15, g4 = (float)(4 * g);
    const float kdf = ((float)kd1 + 28.f * lf) * (1.f / 448.f);
#pragma unroll
    for (int q = 0; q < 4; ++q) {
      const float a = (g4 + q) * kdf;
      const float c = cosr(a), s = sinr(a);
      Br[q] = pk(c, s); Bi[q] = pk(-s, c);
    }
    float c4[4], s4[4];
#pragma unroll
    for (int q = 0; q < 4; ++q) {
      const float a = ((float)kd1 + 28.f * (g4 + q)) * (lf * (1.f / 448.f));
      c4[q] = cosr(a); s4[q] = sinr(a);
    }
    Ar[0] = pk(c4[0], c4[1]); Ar[1] = pk(c4[2], c4[3]);
    Ai[0] = pk(s4[0], s4[1]); Ai[1] = pk(s4[2], s4[3]);
    Ar[2] = Ai[0] ^ NEG2; Ar[3] = Ai[1] ^ NEG2; Ai[2] = Ar[0]; Ai[3] = Ar[1];
    const float af = (4.f * lf + (float)w) * (1.f / 128.f);
#pragma unroll
    for (int q = 0; q < 4; ++q) {
      const float b0 = g4 + (float)(16 * (q >> 1) + 2 * (q & 1));
      const float a0 = af * b0, a1 = a0 + af;
      C1[0][q] = pk(cosr(a0), cosr(a1)); S1[0][q] = pk(sinr(a0), sinr(a1));
      C1[1][q] = C1[0][q] ^ 0x80000000u; S1[1][q] = S1[0][q] ^ 0x80000000u;
    }
    const uint32_t lsg = (l15 & 1) ? NEG2 : 0u;
#pragma unroll
    for (int tb = 0; tb < 2; ++tb) {
      const float bf = (lf + 16.f * tb) * (1.f / 128.f);
#pragma unroll
      for (int q = 0; q < 2; ++q) {
        const float a0 = (4.f * (g4 + 2 * q) + (float)w) * bf, a1 = a0 + 4.f * bf;
        C2[tb][q] = pk(cosr(a0), cosr(a1)); S2[tb][q] = pk(sinr(a0), sinr(a1));
        C2[tb][q + 2] = C2[tb][q] ^ lsg; S2[tb][q + 2] = S2[tb][q] ^ lsg;
      }
    }
  }
  SCHED();
  ST(3);
  const f32x4 zero = {0.f, 0.f, 0.f, 0.f};
  f32x4 tr[2] = {zero, zero}, ti[2] = {zero, zero};
  if (w == 0) k2_fwd<0>(tr, ti, zf, Br, Bi);
  else if (w == 1) k2_fwd<1>(tr, ti, zf, Br, Bi);
  else if (w == 2) k2_fwd<2>(tr, ti, zf, Br, Bi);
  else k2_fwd<3>(tr, ti, zf, Br, Bi);
  {
    const u32x4 Pr = {pk(tr[0][0], tr[0][1]), pk(tr[0][2], tr[0][3]), pk(tr[1][0], tr[1][1]), pk(tr[1][2], tr[1][3])};
    const u32x4 Pi = {pk(ti[0][0], ti[0][1]), pk(ti[0][2], ti[0][3]), pk(ti[1][0], ti[1][1]), pk(ti[1][2], ti[1][3])};
    const u32x4 Nr = Pr ^ NEG2;
    float yr[8], yi[8];
#pragma unroll
    for (int tt = 0; tt < 2; ++tt) {
      f32x4 xr = mfma16(C1[tt], Pr, zero), xi = mfma16(C1[tt], Pi, zero);
      xr = mfma16(S1[tt], Pi, xr); xi = mfma16(S1[tt], Nr, xi);
#pragma unroll
      for (int r = 0; r < 4; ++r) {
        const float a = lo16(wf[tt][r]), b = hi16(wf[tt][r]);
        yr[4 * tt + r] = xr[r] * a - xi[r] * b;
        yi[4 * tt + r] = xr[r] * b + xi[r] * a;
      }
    }
    const u32x4 Yr = {pk(yr[0], yr[1]), pk(yr[2], yr[3]), pk(yr[4], yr[5]), pk(yr[6], yr[7])};
    const u32x4 Yi = {pk(yi[0], yi[1]), pk(yi[2], yi[3]), pk(yi[4], yi[5]), pk(yi[6], yi[7])};
    const u32x4 Ni = Yi ^ NEG2;
#pragma unroll
    for (int tb = 0; tb < 2; ++tb) {
      f32x4 rr = mfma16(Yr, C2[tb], zero), ri = mfma16(Yr, S2[tb], zero);
      rr = mfma16(Ni, S2[tb], rr); ri = mfma16(Yi, C2[tb], ri);
      *(u32x4*)(lds + (w * 2 + tb) * 1024 + lane * 16) = u32x4{pk(rr[0], rr[1]), pk(rr[2], rr[3]), pk(ri[0], ri[1]), pk(ri[2], ri[3])};
    }
  }
  ST(1);
  __syncthreads();
  ST(2);
  u32x4 rf[8];
#pragma unroll
  for (int i = 0; i < 8; ++i) rf[i] = *(const u32x4*)(lds + i * 1024 + lane * 16);
  f32x4 ur[2] = {zero, zero}, ui[2] = {zero, zero};
  if (w == 0) k2_inv<0>(ur, ui, rf, Ar, Ai);
  else if (w == 1) k2_inv<1>(ur, ui, rf, Ar, Ai);
  else if (w == 2) k2_inv<2>(ur, ui, rf, Ar, Ai);
  else k2_inv<3>(ur, ui, rf, Ar, Ai);
  const int slot = (((kd1 >> 2) * 4 + (k1 & 3)) * 4 + (k1 >> 2)) * 4 + (kd1 & 3);
#pragma unroll
  for (int tb = 0; tb < 2; ++tb) {
    u32x4 o = {pk(ur[tb][0], ui[tb][0]), pk(ur[tb][1], ui[tb][1]), pk(ur[tb][2], ui[tb][2]), pk(ur[tb][3], ui[tb][3])};
    BST(o, rws, l15 * 16384 + g * 16, OFF_U + (32 * w + 16 * tb) * 16384 + slot * 64, AUX_X);
  }
  ST_END(ws, 2);
}

__global__ __launch_bounds__(512, 2) void k3(const uint16_t* __restrict__ x, const uint16_t* __restrict__ wd,
                                             char* __restrict__ ws) {
  constexpr int RS = 912;
  __shared__ __attribute__((aligned(16))) char lds[16384];
  ST_BEGIN;
  const int t = threadIdx.x, lane = t & 63, l15 = lane & 15, g = lane >> 4;
  const int w = __builtin_amdgcn_readfirstlane(t >> 6);
  const rsrc_t rws = mk(ws, WS_BYTES);
  if (blockIdx.x >= 128) {
    const int ft = blockIdx.x - 128;
    const rsrc_t rd = mk(wd, D * F * 2);
    BLDS(rd, lds + w * 1024, lane * 16, ft * 14336 + w * 1024);
    if (w < 6) BLDS(rd, lds + 8192 + w * 1024, lane * 16, ft * 14336 + 8192 + w * 1024);
    __syncthreads();
    const int q = (lane >> 2) & 3, p = lane & 3;
    const char* b = lds + (4 * (g >> 1) + q) * 896 + (g & 1) * 32 + p * 8;
#pragma unroll
    for (int i = 0; i < 2; ++i) {
      const int nt = w + 8 * i;
      if (nt < 14) {
        const u32x4 o = trpair(b + nt * 64, b + nt * 64 + 8 * 896);
        BST(o, rws, lane * 16, OFF_WD + (nt * 112 + ft) * 1024, AUX_X);
      }
    }
    ST_END(ws, 3);
    return;
  }
  const int s2 = 16 * (blockIdx.x & 7) + (blockIdx.x >> 3);
  u32x4 uf[4];
  if (w < 4) {
    const int gp = l15 >> 2, r = l15 & 3;
    const uint32_t vo = (g * 4 + gp) * 64 + r * 4, vo3 = (g * 4 + (gp == 3 ? 0 : gp)) * 64 + r * 4;
#pragma unroll
    for (int tt = 0; tt < 4; ++tt)
#pragma unroll
      for (int i = 0; i < 4; ++i)
        uf[tt][i] = __builtin_amdgcn_raw_buffer_load_b32(rws, (tt == 3 ? vo3 : vo) + i * 1024, OFF_U + s2 * 16384 + tt * 4096 + w * 16, 0);
  }
  const int s1 = t >> 5, c = t & 31;
  const bool ok1 = c < 24;
  const rsrc_t rx = mk(x, 2048 * 896);
  const u32x4 v0 = bld(rx, s1 * (128 * 896) + c * 16, s2 * 896);
  const u32x4 v1 = bld(rx, s1 * (128 * 896) + (ok1 ? c + 32 : c) * 16, s2 * 896);
  SCHED();
  if (w < 4) {
    u32x4 Br, Bi, E0, E1;
    const float lf = (float)l15, gf = (float)g;
    const float sf = (float)(128 * l15 + s2) * (1.f / 2048.f);
#pragma unroll
    for (int i = 0; i < 4; ++i) {
      const float a = sf * (4.f * gf + i);
      const float cc = cosr(a), ss = sinr(a);
      Br[i] = pk(cc, -ss); Bi[i] = pk(ss, cc);
    }
    float c0[4], s0[4], c1[4], s1v[4];
    constexpr float SC = 1.f / (2048.f * 448.f);
#pragma unroll
    for (int jj = 0; jj < 4; ++jj) {
      const int kd1 = 4 * jj + g;
      const float wc = (kd1 == 15) ? 0.f : ((kd1 == 0 || kd1 == 14) ? SC : 2.f * SC);
      const float wsn = (kd1 == 15 || kd1 == 0 || kd1 == 14) ? 0.f : -2.f * SC;
      const float kf = (4.f * jj + gf) * (1.f / 28.f);
      const float a0 = lf * kf, a1 = (lf + 16.f) * kf;
      c0[jj] = wc * cosr(a0); s0[jj] = wsn * sinr(a0);
      c1[jj] = wc * cosr(a1); s1v[jj] = wsn * sinr(a1);
    }
    E0 = u32x4{pk(c0[0], c0[1]), pk(c0[2], c0[3]), pk(s0[0], s0[1]), pk(s0[2], s0[3])};
    E1 = u32x4{pk(c1[0], c1[1]), pk(c1[2], c1[3]), pk(s1v[0], s1v[1]), pk(s1v[2], s1v[3])};
    SCHED();
    ST(3);
    const f32x4 zero = {0.f, 0.f, 0.f, 0.f};
    f32x4 vr[4], vi[4];
#pragma unroll
    for (int tt = 0; tt < 4; ++tt) { vr[tt] = mfma16(uf[tt], Br, zero); vi[tt] = mfma16(uf[tt], Bi, zero); }
    f32x4 y0[4], y1[4];
#pragma unroll
    for (int r = 0; r < 4; ++r) {
      u32x4 a = {pk(vr[0][r], vr[1][r]), pk(vr[2][r], vr[3][r]), pk(vi[0][r], vi[1][r]), pk(vi[2][r], vi[3][r])};
      y0[r] = mfma16(a, E0, zero);
      y1[r] = mfma16(a, E1, zero);
    }
#pragma unroll
    for (int rr = 0; rr < 4; ++rr) {
      char* p = lds + (4 * g + rr) * RS + l15 * 32 + 8 * w;
      *(u32x2*)p = u32x2{pk(y0[0][rr], y0[1][rr]), pk(y0[2][rr], y0[3][rr])};
      if (l15 < 12) *(u32x2*)(p + 512) = u32x2{pk(y1[0][rr], y1[1][rr]), pk(y1[2][rr], y1[3][rr])};
    }
  }
  ST(1);
  __syncthreads();
  ST(2);
  const u32x4 y0 = *(const u32x4*)(lds + s1 * RS + c * 16);
  const u32x4 y1 = *(const u32x4*)(lds + s1 * RS + c * 16 + (ok1 ? 512 : 0));
  u32x4 h0, h1;
  float f[16];
#pragma unroll
  for (int q = 0; q < 4; ++q) {
    h0[q] = pk(lo16(v0[q]) + lo16(y0[q]), hi16(v0[q]) + hi16(y0[q]));
    h1[q] = pk(lo16(v1[q]) + lo16(y1[q]), hi16(v1[q]) + hi16(y1[q]));
    f[2 * q] = lo16(h0[q]); f[2 * q + 1] = hi16(h0[q]);
    f[8 + 2 * q] = lo16(h1[q]); f[8 + 2 * q + 1] = hi16(h1[q]);
  }
  BST(h0, rws, s1 * 896 + c * 16, OFF_H + s2 * (16 * 896), AUX_L);
  if (ok1) BST(h1, rws, s1 * 896 + c * 16 + 512, OFF_H + s2 * (16 * 896), AUX_L);
  ln_row(f, ok1);
  const uint32_t vo = (s1 >> 3) * 7168 + (c >> 3) * 1024 + (s1 & 7) * 128 + (((c & 7) ^ ((s1 >> 1) & 7)) << 4);
  BST((u32x4{pk(f[0], f[1]), pk(f[2], f[3]), pk(f[4], f[5]), pk(f[6], f[7])}), rws, vo, OFF_L + s2 * 14336, AUX_L);
  if (ok1) BST((u32x4{pk(f[8], f[9]), pk(f[10], f[11]), pk(f[12], f[13]), pk(f[14], f[15])}), rws, vo, OFF_L + s2 * 14336 + 4096, AUX_L);
  ST_END(ws, 3);
}

#define ALOAD(dst, voff, rs, soff, imm) asm volatile("buffer_load_dwordx4 %0, %1, %2, %3 offen offset:" #imm : "=v"(dst) : "v"(voff), "s"(rs), "s"(soff) : "memory")
__global__ __launch_bounds__(512, 2) void k4(char* __restrict__ ws) {
  __shared__ __attribute__((aligned(16))) char At[112 * 1024];
  ST_BEGIN;
  const int t = threadIdx.x, lane = t & 63;
  const int w = __builtin_amdgcn_readfirstlane(t >> 6);
  const int xcd = blockIdx.x & 7, j = blockIdx.x >> 3;
  const int mt = 4 * (xcd >> 1) + (j & 3), nt = 7 * (xcd & 1) + (j >> 2);
  const int ft = nt * 8 + w;
  const rsrc_t rws = mk(ws, WS_BYTES);
  const uint32_t vo = lane * 16;
  const uint32_t sA = OFF_L + mt * (112 * 1024) + 2 * w * 7168, sW = OFF_WGU + ft * (28 * 1024);
  char* lA = At + 2 * w * 7168;
  u32x4 wf[28];
#define GROUP(n)                                                    \
  {                                                                 \
    BLDS(rws, lA + (n) * 1024, vo, sA + (n) * 1024);                \
    BLDS(rws, lA + 7168 + (n) * 1024, vo, sA + 7168 + (n) * 1024);  \
    const uint32_t so_ = sW + (n) * 4096;                           \
    ALOAD(wf[4 * (n)], vo, rws, so_, 0);                            \
    ALOAD(wf[4 * (n) + 1], vo, rws, so_, 1024);                     \
    ALOAD(wf[4 * (n) + 2], vo, rws, so_, 2048);                     \
    ALOAD(wf[4 * (n) + 3], vo, rws, so_, 3072);                     \
  }
  GROUP(0) GROUP(1) GROUP(2)
  f32x16 acc[4];
#pragma unroll
  for (int q = 0; q < 4; ++q)
#pragma unroll
    for (int e = 0; e < 16; ++e) acc[q][e] = 0.f;
  const int ml = lane & 31, h = lane >> 5;
  const char* abase = At + (ml >> 3) * 7168 + (ml & 7) * 128;
  const int sw = (ml >> 1) & 7;
#define WAITB(seg, N)                                                                                   \
    asm volatile("s_waitcnt vmcnt(" #N ")" : "+v"(wf[4 * seg]), "+v"(wf[4 * seg + 1]), "+v"(wf[4 * seg + 2]), "+v"(wf[4 * seg + 3]) :: "memory"); \
    __builtin_amdgcn_s_barrier();
#define SEG(seg)                                                                                        \
  {                                                                                                     \
    _Pragma("unroll") for (int i = 0; i < 4; ++i) {                                                     \
      _Pragma("unroll") for (int q = 0; q < 4; ++q) {                                                   \
        const u32x4 b = *(const u32x4*)(abase + q * 28672 + seg * 1024 + (((2 * i + h) ^ sw) << 4));    \
        acc[q] = mfma32(wf[4 * seg + i], b, acc[q]);                                                    \
      }                                                                                                 \
    }                                                                                                   \
  }
  ST(1);
  WAITB(0, 12) ST(2); GROUP(3) SEG(0)
  WAITB(1, 12) GROUP(4) SEG(1)
  WAITB(2, 12) GROUP(5) SEG(2)
  WAITB(3, 12) GROUP(6) SEG(3)
  WAITB(4, 12) SEG(4)
  WAITB(5, 6) SEG(5)
  WAITB(6, 0) SEG(6)
  ST(3);
#pragma unroll
  for (int q = 0; q < 4; ++q) {
    float o[8];
#pragma unroll
    for (int e = 0; e < 8; ++e) {
      const float sg = __builtin_amdgcn_rcpf(1.f + __builtin_amdgcn_exp2f(acc[q][e] * -1.44269504088896f));
      o[e] = sg * acc[q][8 + e];
    }
    BST((u32x4{pk(o[0], o[1]), pk(o[2], o[3]), pk(o[4], o[5]), pk(o[6], o[7])}), rws, vo, OFF_HH + ((mt * 4 + q) * 112 + ft) * 1024, AUX_L);
  }
  ST_END(ws, 4);
}

__global__ __launch_bounds__(256, 1) void k5(char* __restrict__ ws, uint16_t* __restrict__ out) {
  constexpr int RS = 272;
  __shared__ __attribute__((aligned(16))) char red[4 * 64 * RS];
  ST_BEGIN;
  const int t = threadIdx.x, lane = t & 63;
  const int w = __builtin_amdgcn_readfirstlane(t >> 6);
  const int xcd = blockIdx.x & 7, j = blockIdx.x >> 3;
  const int mt = 4 * xcd + (j & 3), nt = j >> 2;
  const rsrc_t rws = mk(ws, WS_BYTES);
  const uint32_t vo = lane * 16;
  const uint32_t sH = OFF_HH + (2 * mt * 112 + 28 * w) * 1024, sW = OFF_WD + (2 * nt * 112 + 28 * w) * 1024;
  const int em = t >> 2, en = (t & 3) * 16;
  u32x4 h0, h1;
  {
    const uint32_t hv = em * 896 + en * 2, hs = OFF_H + mt * (64 * 896) + nt * 128;
    ALOAD(h0, hv, rws, hs, 0);
    ALOAD(h1, hv, rws, hs, 16);
  }
  constexpr int PF = 14;
  u32x4 fa[PF][2], fb[PF][2];
#define LD5(s, i)                                         \
  {                                                       \
    ALOAD(fa[s][0], vo, rws, sW + (i) * 1024, 0);         \
    ALOAD(fb[s][0], vo, rws, sH + (i) * 1024, 0);         \
    ALOAD(fa[s][1], vo, rws, sW + (112 + (i)) * 1024, 0); \
    ALOAD(fb[s][1], vo, rws, sH + (112 + (i)) * 1024, 0); \
  }
#pragma unroll
  for (int i = 0; i < PF; ++i) LD5(i, i)
  f32x16 acc[2][2];
#pragma unroll
  for (int a = 0; a < 2; ++a)
#pragma unroll
    for (int b = 0; b < 2; ++b)
#pragma unroll
      for (int e = 0; e < 16; ++e) acc[a][b][e] = 0.f;
#define STEP5(i, N)                                                                                                         \
  {                                                                                                                         \
    constexpr int s = (i) % PF;                                                                                             \
    asm volatile("s_waitcnt vmcnt(" #N ")" : "+v"(fa[s][0]), "+v"(fa[s][1]), "+v"(fb[s][0]), "+v"(fb[s][1]) :: "memory");   \
    _Pragma("unroll") for (int a = 0; a < 2; ++a)                                                                           \
    _Pragma("unroll") for (int b = 0; b < 2; ++b) acc[a][b] = mfma32(fa[s][a], fb[s][b], acc[a][b]);                        \
    if ((i) + PF < 28) LD5(s, (i) + PF)                                                                                     \
  }
  ST(3);
  STEP5(0, 52) STEP5(1, 52) STEP5(2, 52) STEP5(3, 52) STEP5(4, 52) STEP5(5, 52) STEP5(6, 52)
  STEP5(7, 52) STEP5(8, 52) STEP5(9, 52) STEP5(10, 52) STEP5(11, 52) STEP5(12, 52) STEP5(13, 52)
  STEP5(14, 52) STEP5(15, 48) STEP5(16, 44) STEP5(17, 40) STEP5(18, 36) STEP5(19, 32) STEP5(20, 28)
  STEP5(21, 24) STEP5(22, 20) STEP5(23, 16) STEP5(24, 12) STEP5(25, 8) STEP5(26, 4) STEP5(27, 0)
  asm volatile("" : "+v"(h0), "+v"(h1));
  {
    const int ml = lane & 31, h = lane >> 5;
#pragma unroll
    for (int a = 0; a < 2; ++a)
#pragma unroll
      for (int b = 0; b < 2; ++b)
#pragma unroll
        for (int q = 0; q < 4; ++q) {
          f32x4 v = {acc[a][b][4 * q], acc[a][b][4 * q + 1], acc[a][b][4 * q + 2], acc[a][b][4 * q + 3]};
          *(f32x4*)(red + (w * 64 + 32 * b + ml) * RS + (32 * a + 8 * q + 4 * h) * 4) = v;
        }
  }
  ST(1);
  __syncthreads();
  ST(2);
  f32x4 s[4];
#pragma unroll
  for (int q = 0; q < 4; ++q) {
    s[q] = *(const f32x4*)(red + em * RS + (en + 4 * q) * 4);
#pragma unroll
    for (int ww = 1; ww < 4; ++ww) s[q] += *(const f32x4*)(red + (ww * 64 + em) * RS + (en + 4 * q) * 4);
  }
  u32x4 o0, o1;
  o0[0] = pk(s[0][0] + lo16(h0[0]), s[0][1] + hi16(h0[0])); o0[1] = pk(s[0][2] + lo16(h0[1]), s[0][3] + hi16(h0[1]));
  o0[2] = pk(s[1][0] + lo16(h0[2]), s[1][1] + hi16(h0[2])); o0[3] = pk(s[1][2] + lo16(h0[3]), s[1][3] + hi16(h0[3]));
  o1[0] = pk(s[2][0] + lo16(h1[0]), s[2][1] + hi16(h1[0])); o1[1] = pk(s[2][2] + lo16(h1[1]), s[2][3] + hi16(h1[1]));
  o1[2] = pk(s[3][0] + lo16(h1[2]), s[3][1] + hi16(h1[2])); o1[3] = pk(s[3][2] + lo16(h1[3]), s[3][3] + hi16(h1[3]));
  const int gm = 64 * mt + em;
  const int srow = 128 * (gm & 15) + (gm >> 4);
  u32x4* op = (u32x4*)(out + (size_t)srow * D + 64 * nt + en);
  op[0] = o0; op[1] = o1;
  ST_END(ws, 5);
}

extern "C" void kernel_launch(void* const* d_in, const int* in_sizes, int n_in, void* d_out, int out_size,
                              void* d_ws, size_t ws_size, hipStream_t stream) {
  const uint16_t* x = (const uint16_t*)d_in[0];
  const uint16_t* wr = (const uint16_t*)d_in[1];
  const uint16_t* wi = (const uint16_t*)d_in[2];
  const uint16_t* wg = (const uint16_t*)d_in[4];
  const uint16_t* wu = (const uint16_t*)d_in[5];
  const uint16_t* wd = (const uint16_t*)d_in[6];
  char* ws = (char*)d_ws;
  k1<<<256, 512, 0, stream>>>(x, wr, wi, ws);
  k2<<<240 + 196, 256, 0, stream>>>(wg, wu, ws);
  k3<<<128 + 112, 512, 0, stream>>>(x, wd, ws);
  k4<<<224, 512, 0, stream>>>(ws);
  k5<<<224, 256, 0, stream>>>(ws, (uint16_t*)d_out);
}
```

```cpp
#include <hip/hip_runtime.h>
#include <stdint.h>
#include <type_traits>

typedef __attribute__((ext_vector_type(8))) __bf16 bf16x8;
typedef __attribute__((ext_vector_type(2))) __bf16 bf16x2;
typedef __attribute__((ext_vector_type(2))) float f32x2;
typedef __attribute__((ext_vector_type(4))) float f32x4;
typedef __attribute__((ext_vector_type(16))) float f32x16;
typedef __attribute__((ext_vector_type(4))) uint32_t u32x4;
typedef __attribute__((ext_vector_type(2))) uint32_t u32x2;
typedef __attribute__((ext_vector_type(4))) short s16x4;
typedef __amdgpu_buffer_rsrc_t rsrc_t;
#define LDSP __attribute__((address_space(3)))
#define DI __device__ __forceinline__
#define SCHED() __builtin_amdgcn_sched_barrier(0)
#define TIE(x) asm volatile("" : "+v"(x))

#define AUX_X 16
#define AUX_L 16

#define ST(i)
#define ST_END(ws, kid)
#define ST_BEGIN

DI f32x4 mfma16(u32x4 a, u32x4 b, f32x4 c) {
  return __builtin_amdgcn_mfma_f32_16x16x32_bf16(__builtin_bit_cast(bf16x8, a), __builtin_bit_cast(bf16x8, b), c, 0, 0, 0);
}
DI f32x16 mfma32(u32x4 a, u32x4 b, f32x16 c) {
  return __builtin_amdgcn_mfma_f32_32x32x16_bf16(__builtin_bit_cast(bf16x8, a), __builtin_bit_cast(bf16x8, b), c, 0, 0, 0);
}
DI uint32_t pk(float a, float b) {
  f32x2 v = {a, b};
  return __builtin_bit_cast(uint32_t, __builtin_convertvector(v, bf16x2));
}
DI float lo16(uint32_t v) { return __builtin_bit_cast(float, v << 16); }
DI float hi16(uint32_t v) { return __builtin_bit_cast(float, v & 0xffff0000u); }
DI float cosr(float r) { return __builtin_amdgcn_cosf(r); }
DI float sinr(float r) { return __builtin_amdgcn_sinf(r); }
template <int N> DI float ror16(float v) {
  return __builtin_bit_cast(float, __builtin_amdgcn_update_dpp(0, __builtin_bit_cast(int, v), 0x120 + N, 0xf, 0xf, true));
}
DI float sum32(float v) {
  v += ror16<8>(v); v += ror16<4>(v); v += ror16<2>(v); v += ror16<1>(v);
  const uint32_t u = __builtin_bit_cast(uint32_t, v);
  auto s = __builtin_amdgcn_permlane16_swap(u, u, false, false);
  return __builtin_bit_cast(float, (uint32_t)s[0]) + __builtin_bit_cast(float, (uint32_t)s[1]);
}
DI rsrc_t mk(const void* p, uint32_t n) { return __builtin_amdgcn_make_buffer_rsrc((void*)p, 0, n, 0x00020000); }
DI u32x4 bld(rsrc_t r, uint32_t voff, uint32_t soff) { return __builtin_amdgcn_raw_buffer_load_b128(r, voff, soff, 0); }
#define BST(v, r, voff, soff, aux) __builtin_amdgcn_raw_buffer_store_b128(v, r, voff, soff, aux)
#define BLDS(r, ldsptr, voff, soff) __builtin_amdgcn_raw_ptr_buffer_load_lds(r, (LDSP void*)(ldsptr), 16, voff, soff, 0, 0)
DI u32x4 trpair(const char* p0, const char* p1) {
  s16x4 a0 = __builtin_amdgcn_ds_read_tr16_b64_v4i16((LDSP s16x4*)p0);
  s16x4 a1 = __builtin_amdgcn_ds_read_tr16_b64_v4i16((LDSP s16x4*)p1);
  u32x2 b0 = __builtin_bit_cast(u32x2, a0), b1 = __builtin_bit_cast(u32x2, a1);
  return u32x4{b0[0], b0[1], b1[0], b1[1]};
}

template <int I, int N, class Fn> DI void sfor(Fn&& f) {
  if constexpr (I < N) { f(std::integral_constant<int, I>{}); sfor<I + 1, N>(f); }
}

constexpr int D = 448, F = 1792;
constexpr uint32_t ZS = 30720;
constexpr uint32_t US = 16640;
constexpr uint32_t OFF_Z = 0;
constexpr uint32_t OFF_U = 4u << 20;
constexpr uint32_t OFF_MX = 7u << 20;
constexpr uint32_t OFF_FL = OFF_MX + 65536;
constexpr uint32_t OFF_WGU = 8u << 20;
constexpr uint32_t OFF_WD = 12u << 20;
constexpr uint32_t OFF_HH = 14u << 20;
constexpr uint32_t OFF_WF = 22u << 20;
constexpr uint32_t WS_BYTES = 24u << 20;
constexpr uint32_t NEG2 = 0x80008000u;

__global__ __launch_bounds__(512, 2) void k1(const uint16_t* __restrict__ x, const uint16_t* __restrict__ wr,
                                             const uint16_t* __restrict__ wi, const uint16_t* __restrict__ wd,
                                             char* __restrict__ ws) {
  constexpr int RS = 912;
  __shared__ __attribute__((aligned(16))) char lds[16384 + 30720 + 128];
  ST_BEGIN;
  const int t = threadIdx.x, lane = t & 63, l15 = lane & 15, g = lane >> 4;
  const int w = __builtin_amdgcn_readfirstlane(t >> 6);
  const rsrc_t rws = mk(ws, WS_BYTES);
  if (blockIdx.x >= 256) {
    const int kk = blockIdx.x - 256;
    const rsrc_t rd = mk(wd, D * F * 2);
#pragma unroll
    for (int i = 0; i < 4; ++i)
      if (i < 3 || w < 4) BLDS(rd, lds + (i * 8 + w) * 1024, lane * 16, kk * 28672 + (i * 8 + w) * 1024);
    if (t < 4) __builtin_amdgcn_raw_buffer_store_b32(0u, rws, t * 128, OFF_FL + kk * 512, 0);
    __syncthreads();
    const int q = (lane >> 2) & 3, p = lane & 3;
    const char* b = lds + (16 * (g >> 1) + 4 * (g & 1) + q) * 896 + p * 8;
#pragma unroll
    for (int i = 0; i < 4; ++i) {
      const int cf = w + 8 * i;
      if (cf < 28) {
        const u32x4 o = trpair(b + cf * 32, b + cf * 32 + 8 * 896);
        BST(o, rws, lane * 16, OFF_WD + (((cf >> 2) * 56 + kk) * 4 + (cf & 3)) * 1024, AUX_X);
      }
    }
    ST_END(ws, 1);
    return;
  }
  if (blockIdx.x >= 128) {
    const int u = blockIdx.x - 128, k1 = u >> 3, rho = (u >> 1) & 3, tt = u & 1;
    const rsrc_t rr = mk(wr, 2048 * 225 * 2), ri = mk(wi, 2048 * 225 * 2);
    {
      const int i = 2 * w + (lane >> 5);
      const int k = k1 + 16 * (4 * (16 * tt + i) + rho);
      const uint32_t o0 = ((uint32_t)(k * 450) & ~15u) + (lane & 31) * 16;
      const uint32_t o1 = ((uint32_t)(((2048 - k) & 2047) * 450) & ~15u) + (lane & 31) * 16;
      BLDS(rr, lds + w * 1024, o0, 0);
      BLDS(rr, lds + 8192 + w * 1024, o1, 0);
      BLDS(ri, lds + 16384 + w * 1024, o0, 0);
      BLDS(ri, lds + 24576 + w * 1024, o1, 0);
    }
    const int a0 = (k1 * 450) & 15, a1 = (16 - a0) & 15;
    __syncthreads();
#pragma unroll
    for (int n = 0; n < 2; ++n) {
      const int kd1 = w + 8 * n;
      if (kd1 < 15) {
        const int kd = kd1 + 28 * l15;
        const bool mir = kd > 224;
        const char* p = lds + (mir ? 8192 + a1 + 2 * (448 - kd) : a0 + 2 * kd) + g * 2048;
        const uint32_t sg = mir ? 0x80000000u : 0u;
        u32x4 o;
#pragma unroll
        for (int r = 0; r < 4; ++r) {
          const uint32_t re = *(const uint16_t*)(p + r * 512), im = *(const uint16_t*)(p + 16384 + r * 512);
          o[r] = (kd == 0) ? 0u : ((re | (im << 16)) ^ sg);
        }
        BST(o, rws, lane * 16, OFF_WF + (((k1 * 15 + kd1) * 4 + rho) * 2 + tt) * 1024, AUX_X);
      }
    }
    ST_END(ws, 1);
    return;
  }
  const int s2 = blockIdx.x;
  const int s1 = t >> 5, c = t & 31;
  const bool ok1 = c < 24;
  const rsrc_t rx = mk(x, 2048 * 896);
  const u32x4 v0 = bld(rx, s1 * (128 * 896) + c * 16, s2 * 896);
  const u32x4 v1 = bld(rx, s1 * (128 * 896) + (ok1 ? c + 32 : c) * 16, s2 * 896);
  SCHED();
  u32x4 Ec, Es;
  float c2[4], s2v[4];
  {
    const float kf = (float)l15;
    float cc[8], ss[8];
#pragma unroll
    for (int j = 0; j < 8; ++j) {
      const float a = (float)(8 * g + j) * (kf * (1.f / 28.f));
      cc[j] = cosr(a); ss[j] = sinr(-a);
    }
#pragma unroll
    for (int q = 0; q < 4; ++q) { Ec[q] = pk(cc[2 * q], cc[2 * q + 1]); Es[q] = pk(ss[2 * q], ss[2 * q + 1]); }
    const uint32_t m3 = g == 3 ? 0u : ~0u;
    Ec[2] &= m3; Ec[3] &= m3; Es[2] &= m3; Es[3] &= m3;
#pragma unroll
    for (int j = 0; j < 4; ++j) {
      const float a = (float)(128 * (4 * j + g) + s2) * (kf * (1.f / 2048.f));
      c2[j] = cosr(a); s2v[j] = sinr(a);
    }
  }
  float* sgm = (float*)(lds + 14592);
  const int q = (lane >> 2) & 3, p = lane & 3;
  uint32_t b0 = p * RS + 8 * (w & 3) + (8 * g + q) * 32, b1 = b0 + ((g == 3) ? 0 : 128);
  uint32_t mp[4], zw[4], wa0 = s1 * RS + c * 16, wa1 = s1 * RS + (ok1 ? c * 16 + 512 : 896);
  uint32_t si = 14592 + 4 * (c < 2 ? c * 16 + (s1 & 3) * 4 + (s1 >> 2) : c < 6 ? 30 + s1 * 4 + c : 96 + (c & 3));
  uint32_t mo = c == 0 ? s1 * 4 : 0x80000000u;
  float mmul = (c >= 2 && c < 6) ? -28.f : 0.f, madd = c == 1 ? 2048.f : 0.f, k1m = ok1 ? 1.f : 0.f;
#pragma unroll
  for (int i = 0; i < 4; ++i) {
    mp[i] = 14592 + 4 * (l15 == 0 ? 32 + g * 4 + i * 16 : 96);
    const int slot = l15 < 15 ? (4 * g + i) * 15 + l15 : 240;
    zw[i] = 16384 + slot * 128 + ((w ^ (slot & 7)) << 4);
    TIE(mp[i]); TIE(zw[i]); TIE(c2[i]); TIE(s2v[i]);
  }
  TIE(Ec); TIE(Es); TIE(b0); TIE(b1); TIE(wa0); TIE(wa1); TIE(si); TIE(mo); TIE(mmul); TIE(madd); TIE(k1m);
  SCHED();
  {
    *(u32x4*)(lds + wa0) = v0;
    *(u32x4*)(lds + wa1) = v1;
    const bf16x2 one = {(__bf16)1.f, (__bf16)1.f};
    float a0 = 0.f, a1 = 0.f, q0 = 0.f, q1 = 0.f;
#pragma unroll
    for (int i = 0; i < 4; ++i) {
      const uint32_t u0 = v0[i], u1 = v1[i];
      const bf16x2 e0 = __builtin_bit_cast(bf16x2, u0), e1 = __builtin_bit_cast(bf16x2, u1);
      a0 = __builtin_amdgcn_fdot2_f32_bf16(e0, one, a0, false); a1 = __builtin_amdgcn_fdot2_f32_bf16(e1, one, a1, false);
      q0 = __builtin_amdgcn_fdot2_f32_bf16(e0, e0, q0, false); q1 = __builtin_amdgcn_fdot2_f32_bf16(e1, e1, q1, false);
    }
    const float mean = sum32(__builtin_fmaf(a1, k1m, a0)) * (1.f / D);
    const float var = __builtin_fmaxf(sum32(__builtin_fmaf(q1, k1m, q0)) * (1.f / D) - mean * mean, 0.f);
    const float rstd = __builtin_amdgcn_rsqf(var + 1e-5f);
    *(float*)(lds + si) = c == 0 ? rstd : __builtin_fmaf(mean, mmul, madd);
    __builtin_amdgcn_raw_buffer_store_b32(__builtin_bit_cast(uint32_t, mean), rws, mo, OFF_MX + s2 * 64, 0);
  }
  ST(1);
  __syncthreads();
  ST(2);
  {
    f32x4 xr_[4], xi_[4];
    const f32x4 zero = {0.f, 0.f, 0.f, 0.f};
    const f32x4 sc = *(const f32x4*)(sgm + (w >> 2) * 16 + g * 4);
#pragma unroll
    for (int tt = 0; tt < 4; ++tt) {
      const u32x4 a = trpair(lds + b0 + tt * 4 * RS, lds + b1 + tt * 4 * RS);
      xr_[tt] = mfma16(a, Ec, *(const f32x4*)(lds + mp[tt]));
      xi_[tt] = mfma16(a, Es, zero);
    }
    u32x4 Tr, Ti;
    Tr[0] = pk(c2[0] * sc[0], c2[1] * sc[1]); Tr[1] = pk(c2[2] * sc[2], c2[3] * sc[3]);
    Tr[2] = pk(s2v[0] * sc[0], s2v[1] * sc[1]); Tr[3] = pk(s2v[2] * sc[2], s2v[3] * sc[3]);
    Ti[0] = Tr[2] ^ NEG2; Ti[1] = Tr[3] ^ NEG2; Ti[2] = Tr[0]; Ti[3] = Tr[1];
    f32x4 zr[4], zi[4];
#pragma unroll
    for (int r = 0; r < 4; ++r) {
      u32x4 b = {pk(xr_[0][r], xr_[1][r]), pk(xr_[2][r], xr_[3][r]), pk(xi_[0][r], xi_[1][r]), pk(xi_[2][r], xi_[3][r])};
      zr[r] = mfma16(Tr, b, zero);
      zi[r] = mfma16(Ti, b, zero);
    }
#pragma unroll
    for (int rr = 0; rr < 4; ++rr)
      *(u32x4*)(lds + zw[rr]) = u32x4{pk(zr[0][rr], zi[0][rr]), pk(zr[1][rr], zi[1][rr]), pk(zr[2][rr], zi[2][rr]), pk(zr[3][rr], zi[3][rr])};
  }
  __syncthreads();
  ST(3);
#pragma unroll
  for (int i = 0; i < 4; ++i)
    if (i < 3 || w < 6) {
      const u32x4 o = *(const u32x4*)(lds + 16384 + (i * 8 + w) * 1024 + lane * 16);
      BST(o, rws, lane * 16, OFF_Z + s2 * ZS + (i * 8 + w) * 1024, AUX_X);
    }
  ST_END(ws, 1);
}

template <int N, bool CONST_IS_B> DI void rot_acc(f32x4& tr, f32x4& ti, u32x4 d, u32x4 P, u32x4 Q, u32x4 nP, u32x4 nQ) {
  const u32x4 cr = (N == 0) ? P : (N == 1) ? Q : (N == 2) ? nP : nQ;
  const u32x4 ci = (N == 0) ? Q : (N == 1) ? nP : (N == 2) ? nQ : P;
  if (CONST_IS_B) { tr = mfma16(d, cr, tr); ti = mfma16(d, ci, ti); }
  else { tr = mfma16(cr, d, tr); ti = mfma16(ci, d, ti); }
}
template <int RHO> DI void k2_fwd(f32x4 (&tr)[2], f32x4 (&ti)[2], const u32x4 (&zf)[8], u32x4 P, u32x4 Q) {
  const u32x4 nP = P ^ NEG2, nQ = Q ^ NEG2;
#pragma unroll
  for (int tb = 0; tb < 2; ++tb) rot_acc<0, true>(tr[tb], ti[tb], zf[tb], P, Q, nP, nQ);
#pragma unroll
  for (int tb = 0; tb < 2; ++tb) rot_acc<(RHO) & 3, true>(tr[tb], ti[tb], zf[2 + tb], P, Q, nP, nQ);
#pragma unroll
  for (int tb = 0; tb < 2; ++tb) rot_acc<(2 * RHO) & 3, true>(tr[tb], ti[tb], zf[4 + tb], P, Q, nP, nQ);
#pragma unroll
  for (int tb = 0; tb < 2; ++tb) rot_acc<(3 * RHO) & 3, true>(tr[tb], ti[tb], zf[6 + tb], P, Q, nP, nQ);
}
template <int KS> DI void k2_inv(f32x4 (&ur)[2], f32x4 (&ui)[2], const u32x4 (&rf)[8], u32x4 P, u32x4 Q) {
  const u32x4 nP = P ^ NEG2, nQ = Q ^ NEG2;
#pragma unroll
  for (int tb = 0; tb < 2; ++tb) rot_acc<0, false>(ur[tb], ui[tb], rf[tb], P, Q, nP, nQ);
#pragma unroll
  for (int tb = 0; tb < 2; ++tb) rot_acc<(4 - (KS & 3)) & 3, false>(ur[tb], ui[tb], rf[2 + tb], P, Q, nP, nQ);
#pragma unroll
  for (int tb = 0; tb < 2; ++tb) rot_acc<(4 - ((2 * KS) & 3)) & 3, false>(ur[tb], ui[tb], rf[4 + tb], P, Q, nP, nQ);
#pragma unroll
  for (int tb = 0; tb < 2; ++tb) rot_acc<(4 - ((3 * KS) & 3)) & 3, false>(ur[tb], ui[tb], rf[6 + tb], P, Q, nP, nQ);
}

__global__ __launch_bounds__(256, 2) void k2(const uint16_t* __restrict__ wg, const uint16_t* __restrict__ wu,
                                             char* __restrict__ ws) {
  __shared__ __attribute__((aligned(16))) char lds[16384];
  ST_BEGIN;
  const int t = threadIdx.x, lane = t & 63, l15 = lane & 15, g = lane >> 4;
  const int w = __builtin_amdgcn_readfirstlane(t >> 6);
  const rsrc_t rws = mk(ws, WS_BYTES);
  if (blockIdx.x >= 240) {
    const int u = blockIdx.x - 240, nt = u / 14, kp = u % 14;
    const rsrc_t rg = mk(wg, D * F * 2), ru = mk(wu, D * F * 2);
    const uint32_t vo = (8 * w + g) * (F * 2) + l15 * 16, so = kp * 32 * (F * 2) + nt * 256;
    BLDS(rg, lds + w * 2048, vo, so);
    BLDS(rg, lds + w * 2048 + 1024, vo, so + 4 * F * 2);
    BLDS(ru, lds + 8192 + w * 2048, vo, so);
    BLDS(ru, lds + 8192 + w * 2048 + 1024, vo, so + 4 * F * 2);
    __syncthreads();
    const int q = (lane >> 2) & 3, p = lane & 3;
    const char* b = lds + (g & 1) * 8192 + (8 * (g >> 1) + q) * 256 + p * 8 + w * 64;
#pragma unroll
    for (int i = 0; i < 2; ++i)
#pragma unroll
      for (int ks = 0; ks < 2; ++ks) {
        const u32x4 o = trpair(b + ks * 4096 + i * 32, b + ks * 4096 + i * 32 + 1024);
        BST(o, rws, lane * 16, OFF_WGU + ((nt * 8 + 2 * w + i) * 28 + 2 * kp + ks) * 1024, AUX_X);
      }
    ST_END(ws, 2);
    return;
  }
  const int xcd = blockIdx.x & 7, j = blockIdx.x >> 3;
  const int k1 = 2 * xcd + (j >= 15), kd1 = j - (j >= 15 ? 15 : 0);
  u32x4 zf[8], wf[2], zx[2];
  {
    const int slot = k1 * 15 + kd1;
    const uint32_t so = OFF_Z + slot * 128, vz = l15 * ZS + ((g ^ (slot & 7)) << 4);
#pragma unroll
    for (int i = 0; i < 8; ++i) zf[i] = bld(rws, vz, so + i * (16 * ZS));
    zx[0] = bld(rws, vz ^ 64, so + (2 * w) * (16 * ZS));
    zx[1] = bld(rws, vz ^ 64, so + (2 * w + 1) * (16 * ZS));
    wf[0] = bld(rws, lane * 16, OFF_WF + ((k1 * 15 + kd1) * 4 + w) * 2048);
    wf[1] = bld(rws, lane * 16, OFF_WF + ((k1 * 15 + kd1) * 4 + w) * 2048 + 1024);
  }
  SCHED();
  u32x4 Br, Bi, Ar, Ai, C1[2], S1[2], C2[2], S2[2];
  {
    const float lf = (float)l15, g4 = (float)(4 * g);
    const float kdf = ((float)kd1 + 28.f * lf) * (1.f / 448.f);
#pragma unroll
    for (int q = 0; q < 4; ++q) {
      const float a = (g4 + q) * kdf;
      const float c = cosr(a), s = sinr(a);
      Br[q] = pk(c, s); Bi[q] = pk(-s, c);
    }
    float c4[4], s4[4];
#pragma unroll
    for (int q = 0; q < 4; ++q) {
      const float a = ((float)kd1 + 28.f * (g4 + q)) * (lf * (1.f / 448.f));
      c4[q] = cosr(a); s4[q] = sinr(a);
    }
    Ar[0] = pk(c4[0], c4[1]); Ar[1] = pk(c4[2], c4[3]);
    Ai[0] = pk(s4[0], s4[1]); Ai[1] = pk(s4[2], s4[3]);
    Ar[2] = Ai[0] ^ NEG2; Ar[3] = Ai[1] ^ NEG2; Ai[2] = Ar[0]; Ai[3] = Ar[1];
    const float af = (4.f * lf + (float)w) * (1.f / 128.f);
#pragma unroll
    for (int q = 0; q < 4; ++q) {
      const float b0 = g4 + (float)(16 * (q >> 1) + 2 * (q & 1));
      const float a0 = af * b0, a1 = a0 + af;
      C1[0][q] = pk(cosr(a0), cosr(a1)); S1[0][q] = pk(sinr(a0), sinr(a1));
      C1[1][q] = C1[0][q] ^ 0x80000000u; S1[1][q] = S1[0][q] ^ 0x80000000u;
    }
    const uint32_t lsg = (l15 & 1) ? NEG2 : 0u;
#pragma unroll
    for (int tb = 0; tb < 2; ++tb) {
      const float bf = (lf + 16.f * tb) * (1.f / 128.f);
#pragma unroll
      for (int q = 0; q < 2; ++q) {
        const float a0 = (4.f * (g4 + 2 * q) + (float)w) * bf, a1 = a0 + 4.f * bf;
        C2[tb][q] = pk(cosr(a0), cosr(a1)); S2[tb][q] = pk(sinr(a0), sinr(a1));
        C2[tb][q + 2] = C2[tb][q] ^ lsg; S2[tb][q + 2] = S2[tb][q] ^ lsg;
      }
    }
  }
  SCHED();
  ST(3);
  const f32x4 zero = {0.f, 0.f, 0.f, 0.f};
  f32x4 tr[2] = {zero, zero}, ti[2] = {zero, zero};
  if (w == 0) k2_fwd<0>(tr, ti, zf, Br, Bi);
  else if (w == 1) k2_fwd<1>(tr, ti, zf, Br, Bi);
  else if (w == 2) k2_fwd<2>(tr, ti, zf, Br, Bi);
  else k2_fwd<3>(tr, ti, zf, Br, Bi);
  {
    const u32x4 Pr = {pk(tr[0][0], tr[0][1]), pk(tr[0][2], tr[0][3]), pk(tr[1][0], tr[1][1]), pk(tr[1][2], tr[1][3])};
    const u32x4 Pi = {pk(ti[0][0], ti[0][1]), pk(ti[0][2], ti[0][3]), pk(ti[1][0], ti[1][1]), pk(ti[1][2], ti[1][3])};
    const u32x4 Nr = Pr ^ NEG2;
    float yr[8], yi[8];
#pragma unroll
    for (int tt = 0; tt < 2; ++tt) {
      f32x4 xr = mfma16(C1[tt], Pr, zero), xi = mfma16(C1[tt], Pi, zero);
      xr = mfma16(S1[tt], Pi, xr); xi = mfma16(S1[tt], Nr, xi);
#pragma unroll
      for (int r = 0; r < 4; ++r) {
        const float a = lo16(wf[tt][r]), b = hi16(wf[tt][r]);
        yr[4 * tt + r] = xr[r] * a - xi[r] * b;
        yi[4 * tt + r] = xr[r] * b + xi[r] * a;
      }
    }
    const u32x4 Yr = {pk(yr[0], yr[1]), pk(yr[2], yr[3]), pk(yr[4], yr[5]), pk(yr[6], yr[7])};
    const u32x4 Yi = {pk(yi[0], yi[1]), pk(yi[2], yi[3]), pk(yi[4], yi[5]), pk(yi[6], yi[7])};
    const u32x4 Ni = Yi ^ NEG2;
#pragma unroll
    for (int tb = 0; tb < 2; ++tb) {
      f32x4 rr = mfma16(Yr, C2[tb], zero), ri = mfma16(Yr, S2[tb], zero);
      rr = mfma16(Ni, S2[tb], rr); ri = mfma16(Yi, C2[tb], ri);
      *(u32x4*)(lds + (w * 2 + tb) * 1024 + lane * 16) = u32x4{pk(rr[0], rr[1]), pk(rr[2], rr[3]), pk(ri[0], ri[1]), pk(ri[2], ri[3])};
    }
  }
  ST(1);
  __syncthreads();
  ST(2);
  u32x4 rf[8];
#pragma unroll
  for (int i = 0; i < 8; ++i) rf[i] = *(const u32x4*)(lds + i * 1024 + lane * 16);
  f32x4 ur[2], ui[2];
#pragma unroll
  for (int tb = 0; tb < 2; ++tb)
#pragma unroll
    for (int r = 0; r < 4; ++r) { ur[tb][r] = lo16(zx[tb][r]); ui[tb][r] = hi16(zx[tb][r]); }
  if (w == 0) k2_inv<0>(ur, ui, rf, Ar, Ai);
  else if (w == 1) k2_inv<1>(ur, ui, rf, Ar, Ai);
  else if (w == 2) k2_inv<2>(ur, ui, rf, Ar, Ai);
  else k2_inv<3>(ur, ui, rf, Ar, Ai);
  const uint32_t su = OFF_U + 32 * w * US + ((kd1 >> 2) * 4 + (k1 >> 2)) * 1024 + ((k1 & 3) * 4 + (kd1 & 3)) * 64;
#pragma unroll
  for (int tb = 0; tb < 2; ++tb) {
    u32x4 o = {pk(ur[tb][0], ur[tb][1]), pk(ur[tb][2], ur[tb][3]), pk(ui[tb][0], ui[tb][1]), pk(ui[tb][2], ui[tb][3])};
    BST(o, rws, l15 * US + ((g ^ (k1 & 3)) << 4), su + 16 * tb * US, AUX_X);
  }
  ST_END(ws, 2);
}

#define ALOAD(dst, voff, rs, soff) asm volatile("buffer_load_dwordx4 %0, %1, %2, %3 offen" : "=v"(dst) : "v"(voff), "s"(rs), "s"(soff) : "memory")
#define ALOADC(dst, voff, rs, soff) asm volatile("buffer_load_dwordx4 %0, %1, %2, %3 offen sc1" : "=v"(dst) : "v"(voff), "s"(rs), "s"(soff) : "memory")
#define APOLL(dst, voff, rs, soff) asm volatile("buffer_load_dword %0, %1, %2, %3 offen sc1" : "=v"(dst) : "v"(voff), "s"(rs), "s"(soff) : "memory")
#define ADMA4(ldsa, voff, rs, soff) asm volatile("s_mov_b32 m0, %0\n\ts_nop 0\n\tbuffer_load_dwordx4 %1, %2, %3 offen lds\n\tbuffer_load_dwordx4 %1, %2, %3 offen offset:1024 lds\n\t" \
    "buffer_load_dwordx4 %1, %2, %3 offen offset:2048 lds\n\tbuffer_load_dwordx4 %1, %2, %3 offen offset:3072 lds" ::"s"(ldsa), "v"(voff), "s"(rs), "s"(soff) : "memory")
#define ALOAD4(d, voff, rs, soff) asm volatile("buffer_load_dwordx4 %0, %4, %5, %6 offen\n\tbuffer_load_dwordx4 %1, %4, %5, %6 offen offset:1024\n\t" \
    "buffer_load_dwordx4 %2, %4, %5, %6 offen offset:2048\n\tbuffer_load_dwordx4 %3, %4, %5, %6 offen offset:3072" \
    : "=&v"((d)[0]), "=&v"((d)[1]), "=&v"((d)[2]), "=&v"((d)[3]) : "v"(voff), "s"(rs), "s"(soff) : "memory")
#define VMW(N) asm volatile("s_waitcnt vmcnt(%0)" ::"n"(N) : "memory")
#define ALOADT(dst, voff, rs, soff, tie) asm volatile("buffer_load_dwordx4 %0, %2, %3, %4 offen" : "=&v"(dst), "+v"(tie) : "v"(voff), "s"(rs), "s"(soff) : "memory")
#define ARELOADC(dst, voff, rs, soff) asm volatile("buffer_load_dwordx4 %0, %1, %2, %3 offen sc1" : "+v"(dst) : "v"(voff), "s"(rs), "s"(soff) : "memory")
constexpr int LK = 512, RED = 272;
constexpr int L_SMALL = 32 * 16 * RED;
constexpr uint32_t SENT = 0xffffffffu;
DI uint32_t umax(uint32_t a, uint32_t b) { return a > b ? a : b; }
DI uint32_t umin(uint32_t a, uint32_t b) { return a < b ? a : b; }
DI uint32_t max3u(uint32_t a, uint32_t b, uint32_t c) { return umax(umax(a, b), c); }

constexpr int LAG = 10;
constexpr int step_q(int i) { const int u = i % 56; return 2 * (i / 56) + (u < LAG ? 0 : u >= 56 - LAG ? 1 : ((u - LAG) & 1) ^ 1); }
constexpr int step_k(int i) { const int u = i % 56; return u < LAG ? u : u >= 56 - LAG ? u - 28 : ((u - LAG) & 1) ? LAG + (u - LAG) / 2 : (u - LAG) / 2; }
#define NE 16
constexpr int late(int i) { return (i >= 67 && i <= 101 && i % 2 == 1) ? (i - 67) / 2 : (i >= 103 && i <= 110) ? i - 85 : -1; }
constexpr int late_item(int i) { return late(i) < 0 || NE + late(i) >= 42 ? -1 : NE + late(i); }
constexpr int LATE_END = NE + 26 < 42 ? NE + 26 : 42;
constexpr int vm_op(int i) {
  return (i < 24 + 2 * NE && i % 2 == 0) || (i <= 7) || i == 55 || i == 65 || i == 66 || late_item(i) >= 0 || i == 111;
}
constexpr int vm_ops(int a, int b) { int n = 0; for (int i = a; i < b; ++i) n += vm_op(i); return n; }

__global__ __launch_bounds__(512, 2) void k3(char* __restrict__ ws, uint16_t* __restrict__ out) {
  __shared__ __attribute__((aligned(16))) char lds[L_SMALL + 1024 + 16];
  ST_BEGIN;
  const int t = threadIdx.x, lane = t & 63, l15 = lane & 15, g = lane >> 4;
  const int w = __builtin_amdgcn_readfirstlane(t >> 6);
  const int xcd = blockIdx.x & 7, jb = blockIdx.x >> 3;
  const int mt = 4 * (xcd >> 1) + (jb & 3), rp = xcd & 1, ct = jb >> 2, nt = 7 * rp + ct;
  const int sw = w;
  const rsrc_t rws = mk(ws, WS_BYTES);
  const uint32_t vo = lane * 16;
  float* rstd_s = (float*)(lds + L_SMALL);
  uint32_t* cnt_s = (uint32_t*)(lds + L_SMALL + 1024);
  char* reg = lds + sw * 16384;
  uint32_t mx;
  auto dma = [&]() {
    const uint32_t su = OFF_U + (8 * mt + sw) * US;
#pragma unroll
    for (int n = 0; n < 4; ++n) {
      const uint32_t o = n * 4096;
      ADMA4((uint32_t)(uintptr_t)(LDSP char*)lds + sw * 16384 + o, vo, rws, su + o);
    }
  };
  asm volatile("buffer_load_dword %0, %1, %2, %3 offen" : "=v"(mx) : "v"(l15 * 4), "s"(rws), "s"(OFF_MX + (8 * mt + sw) * 64) : "memory");
  if (w < 4) dma();
  u32x4 wf[28];
  const uint32_t sW = OFF_WGU + (nt * 8 + w) * (28 * 1024);
  auto wad = [&](int i) { return sW + i * 1024; };
  const uint32_t sP = OFF_HH + (mt * 4 * 112 + nt * 8 + w) * 1024;
  cnt_s[0] = 0;
  {
    u32x4 Br, Bi, E0, E1;
    const float lf = (float)l15, gf = (float)g;
    const float sf = (float)(128 * l15 + 8 * mt + sw) * (1.f / 2048.f);
    const float ph = (g & 1) ? 0.25f : 0.f;
    float cc[4], ss[4];
#pragma unroll
    for (int i = 0; i < 4; ++i) {
      const float a = sf * (4.f * gf + i) + ph;
      cc[i] = cosr(a); ss[i] = sinr(a);
    }
    const uint32_t mo = (g & 1) ? NEG2 : 0u;
    Br[0] = pk(cc[0], cc[1]); Br[1] = pk(cc[2], cc[3]);
    Bi[0] = pk(ss[0], ss[1]); Bi[1] = pk(ss[2], ss[3]);
    Br[2] = Bi[0] ^ mo ^ NEG2; Br[3] = Bi[1] ^ mo ^ NEG2;
    Bi[2] = Br[0] ^ mo; Bi[3] = Br[1] ^ mo;
    float c0[4], s0[4], c1[4], s1v[4];
    constexpr float SC = 1.f / (2048.f * 448.f);
    const float z1 = (l15 < 12) ? 1.f : 0.f;
#pragma unroll
    for (int jj = 0; jj < 4; ++jj) {
      const int kd1 = 4 * jj + g;
      const float wc = (kd1 == 15) ? 0.f : ((kd1 == 0 || kd1 == 14) ? SC : 2.f * SC);
      const float wsn = (kd1 == 15 || kd1 == 0 || kd1 == 14) ? 0.f : -2.f * SC;
      const float kf = (4.f * jj + gf) * (1.f / 28.f);
      const float a0 = lf * kf, a1 = (lf + 16.f) * kf;
      c0[jj] = wc * cosr(a0); s0[jj] = wsn * sinr(a0);
      c1[jj] = (wc * z1) * cosr(a1); s1v[jj] = (wsn * z1) * sinr(a1);
    }
    E0 = u32x4{pk(c0[0], c0[1]), pk(c0[2], c0[3]), pk(s0[0], s0[1]), pk(s0[2], s0[3])};
    E1 = u32x4{pk(c1[0], c1[1]), pk(c1[2], c1[3]), pk(s1v[0], s1v[1]), pk(s1v[2], s1v[3])};
    const int q = (lane >> 2) & 3, p = lane & 3;
    uint32_t ua[2][2][4];
#pragma unroll
    for (int j = 0; j < 4; ++j)
#pragma unroll
      for (int hf = 0; hf < 2; ++hf) {
        ua[0][hf][j] = sw * 16384 + g * 1024 + (q * 4 + p) * 64 + (q ^ j) * 16 + ((g & 1) ^ hf) * 8;
        ua[1][hf][j] = ua[0][hf][j] - (p == 3 ? 192 : 0);
        TIE(ua[0][hf][j]); TIE(ua[1][hf][j]);
      }
    TIE(Br); TIE(Bi); TIE(E0); TIE(E1);
    if (w >= 4) dma();
    __builtin_amdgcn_s_barrier();
    constexpr int WE = 4;
#pragma unroll
    for (int i = 0; i < WE; ++i) ALOAD(wf[i], vo, rws, wad(i));
    SCHED();
    ST(1);
    asm volatile("s_waitcnt vmcnt(%1)" : "+v"(mx) : "n"(WE) : "memory");
    ST(2);
    rstd_s[128 + 16 * sw + l15] = __builtin_bit_cast(float, mx);
    u32x4 uf[4][4];
    auto rd = [&](int j) {
#pragma unroll
      for (int tt = 0; tt < 4; ++tt) uf[j][tt] = trpair(lds + ua[tt == 3][0][j] + tt * 4096, lds + ua[tt == 3][1][j] + tt * 4096);
    };
    rd(0); rd(1);
    const f32x4 zero = {0.f, 0.f, 0.f, 0.f};
    f32x4 gram = zero;
    u32x2 P0[4][4], P1[4][4];
    char* lw = reg + g * (4 * LK) + l15 * 16;
#pragma unroll
    for (int j = 0; j < 4; ++j) {
      f32x4 vr[4], vi[4];
#pragma unroll
      for (int tt = 0; tt < 4; ++tt) {
        if (tt == 2) ALOADT(wf[WE + 3 * j], vo, rws, wad(WE + 3 * j), uf[j][tt]);
        vr[tt] = mfma16(uf[j][tt], Br, zero); vi[tt] = mfma16(uf[j][tt], Bi, zero);
      }
      if (j < 2) rd(j + 2);
      f32x4 y0[4], y1[4];
#pragma unroll
      for (int r = 0; r < 4; ++r) {
        u32x4 a = {pk(vr[0][r], vr[1][r]), pk(vr[2][r], vr[3][r]), pk(vi[0][r], vi[1][r]), pk(vi[2][r], vi[3][r])};
        if (!(r & 1)) ALOADT(wf[WE + 3 * j + 1 + (r >> 1)], vo, rws, wad(WE + 3 * j + 1 + (r >> 1)), a);
        y0[r] = mfma16(E0, a, zero);
        y1[r] = mfma16(E1, a, zero);
      }
#pragma unroll
      for (int rr = 0; rr < 4; ++rr) {
        P0[j][rr] = u32x2{pk(y0[0][rr], y0[1][rr]), pk(y0[2][rr], y0[3][rr])};
        P1[j][rr] = u32x2{pk(y1[0][rr], y1[1][rr]), pk(y1[2][rr], y1[3][rr])};
      }
      if (j & 1) {
#pragma unroll
        for (int rr = 0; rr < 4; ++rr) {
          const u32x4 ca = {P0[j - 1][rr][0], P0[j - 1][rr][1], P0[j][rr][0], P0[j][rr][1]};
          const u32x4 cb = {P1[j - 1][rr][0], P1[j - 1][rr][1], P1[j][rr][0], P1[j][rr][1]};
          *(u32x4*)(lw + (j >> 1) * 256 + rr * LK) = ca;
          *(u32x4*)(lw + (j >> 1) * 256 + (16 + rr) * LK) = cb;
          gram = mfma16(ca, ca, gram);
          gram = mfma16(cb, cb, gram);
        }
      }
    }
    const float dg = (l15 & 2) ? ((l15 & 1) ? gram[3] : gram[2]) : ((l15 & 1) ? gram[1] : gram[0]);
    if (g == (l15 >> 2)) rstd_s[16 * sw + l15] = __builtin_amdgcn_rsqf(dg * (1.f / D) + 1e-5f);
  }
  const int ml = lane & 31;
  uint32_t bo[2];
  bo[0] = (ml >> 4) * 16384 + (lane >> 5) * 256 + (ml & 15) * 16;
  bo[1] = bo[0] + 65536;
  TIE(bo[0]); TIE(bo[1]);
  asm volatile("s_waitcnt lgkmcnt(0)" ::: "memory");
  __builtin_amdgcn_s_barrier();
  __builtin_amdgcn_s_setprio(3);
  SCHED();
  ST(3);

  const uint32_t sD = OFF_WD + (ct * 56 + 7 * w) * 4096;
  const uint32_t sH = OFF_HH + (mt * 4 * 112 + 14 * w) * 1024 + rp * 256, vH = g * 512 + l15 * 16;
  const uint32_t sF = OFF_FL + mt * (14 * 128);
  const uint32_t vF = min((14 * w >> 3) + (lane & 3), (14 * w + 13) >> 3) * 128;
  u32x4 wd[28], hh[4][7];
  uint32_t pf;
  f32x16 acc[2];
  float o[8], rs[2], c1[2];
  constexpr int PB = 4;
  u32x4 bf[PB];
  auto glu = [&](int p, int e) {
    const float sg = __builtin_amdgcn_rcpf(1.f + __builtin_amdgcn_exp2f(acc[p][e] * c1[p]));
    o[e] = (acc[p][8 + e] * rs[p]) * sg;
    TIE(o[e]);
  };
  auto publish = [&](int q) {
    BST((u32x4{umin(pk(o[0], o[1]), SENT - 1), umin(pk(o[2], o[3]), SENT - 1), umin(pk(o[4], o[5]), SENT - 1), umin(pk(o[6], o[7]), SENT - 1)}),
        rws, vo, sP + q * (112 * 1024), 16);
  };
#pragma unroll
  for (int i = 0; i < PB - 1; ++i) bf[i] = *(const u32x4*)(lds + bo[0] + i * LK);
  sfor<0, 112>([&](auto I) {
    constexpr int i = I, u = i % 56, q = step_q(i), ks = step_k(i);
    if constexpr (i + PB - 1 < 112) {
      constexpr int n = i + PB - 1;
      bf[n % PB] = *(const u32x4*)(lds + bo[n / 56] + (step_q(n) & 1) * 32768 + step_k(n) * LK);
    }
    if constexpr (q == 0) asm volatile("s_waitcnt vmcnt(%1)" : "+v"(wf[ks]) : "n"(ks < 16 ? 15 - ks + vm_ops(0, i) : vm_ops(2 * (ks - 16) + 1, i)) : "memory");
    if constexpr (i == 28) ST(4);
    if constexpr (i == 56) ST(5);
    if constexpr (i == 36) __builtin_amdgcn_s_setprio(2);
    if constexpr (i == 76) __builtin_amdgcn_s_setprio(1);
    if constexpr (late_item(i) == 28) {
      ST(6);
      TIE(acc[0]); TIE(acc[1]);
      asm volatile("s_waitcnt vmcnt(%1)" : "+v"(pf) : "n"(vm_ops(67, i)) : "memory");
      for (int spin = 0; __builtin_amdgcn_ballot_w64(pf == 0) != 0 && spin < (1 << 14); ++spin) {
        __builtin_amdgcn_s_sleep(1);
        APOLL(pf, vF, rws, sF);
        asm volatile("s_waitcnt vmcnt(0)" : "+v"(pf)::"memory");
      }
      ST(7);
    }
    if constexpr (ks == 0) {
      const f32x16 z = {0.f, 0.f, 0.f, 0.f, 0.f, 0.f, 0.f, 0.f, 0.f, 0.f, 0.f, 0.f, 0.f, 0.f, 0.f, 0.f};
      acc[q & 1] = mfma32(wf[ks], bf[i % PB], z);
    } else {
      acc[q & 1] = mfma32(wf[ks], bf[i % PB], acc[q & 1]);
    }
    if constexpr (ks == 25) { rs[q & 1] = rstd_s[32 * q + ml]; }
    if constexpr (ks == 27) { c1[q & 1] = rs[q & 1] * -1.44269504088896f; }
    if constexpr (u >= 47 && u <= 54) glu(0, u - 47);
    if constexpr (i >= 57 && i <= 64) glu(1, i - 57);
    if constexpr (i == 32) {
      TIE(acc[0]); TIE(acc[1]);
      if (lane == 0) {
        const uint32_t old = __hip_atomic_fetch_add(cnt_s, 1u, __ATOMIC_RELAXED, __HIP_MEMORY_SCOPE_WORKGROUP);
        if (old == 7) __builtin_amdgcn_raw_buffer_store_b32(1u, rws, 0, sF + nt * 128, 16);
      }
    }
    if constexpr (i <= 22 && i % 2 == 0) ALOAD(wf[16 + i / 2], vo, rws, wad(16 + i / 2));
    if constexpr (i <= 7 && i % 2 == 1) BST((u32x4{SENT, SENT, SENT, SENT}), rws, vo, sP + (i / 2) * (112 * 1024), 16);
    if constexpr (i >= 24 && i < 24 + 2 * NE && i % 2 == 0) ALOAD(wd[(i - 24) / 2], vo, rws, sD + ((i - 24) / 2) * 1024);
    if constexpr (i == 55) publish(0);
    if constexpr (i == 65) publish(1);
    if constexpr (i == 66) APOLL(pf, vF, rws, sF);
    constexpr int li = late_item(i);
    if constexpr (li >= 0 && li < 28) ALOAD(wd[li], vo, rws, sD + li * 1024);
    if constexpr (li >= 28) ALOADC(hh[(li - 28) / 7][(li - 28) % 7], vH, rws, sH + ((li - 28) / 7) * (112 * 1024) + ((li - 28) % 7) * 2048);
    if constexpr (i == 111) publish(2);
    SCHED();
  });
  ST(8);
#pragma unroll
  for (int e = 0; e < 8; ++e) glu(1, e);
  publish(3);
  __builtin_amdgcn_s_setprio(0);
#pragma unroll
  for (int li = LATE_END; li < 42; ++li) ALOADC(hh[1][li - 35], vH, rws, sH + 112 * 1024 + (li - 35) * 2048);
  auto fetch = [&](int q) {
#pragma unroll
    for (int i = 0; i < 7; ++i) ALOADC(hh[q][i], vH, rws, sH + q * (112 * 1024) + i * 2048);
  };
  const int er = 2 * w + (lane >> 5), ec = 64 * ct + 2 * (lane & 31);
  uint32_t rc[4];
  float rm[4];
#pragma unroll
  for (int q = 0; q < 4; ++q) {
    rc[q] = *(const uint32_t*)(lds + (2 * q + rp) * 16384 + (ec >> 4) * LK + ((ec >> 3) & 1) * 256 + er * 16 + (ec & 7) * 2);
    rm[q] = rstd_s[128 + 32 * q + 16 * rp + er];
  }
  f32x4 a2[4][4];
  auto gemm2 = [&](int q) {
#pragma unroll
    for (int i = 0; i < 7; ++i) TIE(hh[q][i]);
    for (int spin = 0; spin < (1 << 14); ++spin) {
      uint32_t m = 0;
#pragma unroll
      for (int i = 0; i < 7; ++i) m = max3u(max3u(m, hh[q][i][0], hh[q][i][1]), hh[q][i][2], hh[q][i][3]);
      if (__builtin_amdgcn_ballot_w64(m == SENT) == 0) break;
#pragma unroll
      for (int i = 0; i < 7; ++i) ARELOADC(hh[q][i], vH, rws, sH + q * (112 * 1024) + i * 2048);
      VMW(0);
#pragma unroll
      for (int i = 0; i < 7; ++i) TIE(hh[q][i]);
    }
#pragma unroll
    for (int cf = 0; cf < 4; ++cf) a2[q][cf] = f32x4{0.f, 0.f, 0.f, 0.f};
#pragma unroll
    for (int i = 0; i < 7; ++i)
#pragma unroll
      for (int cf = 0; cf < 4; ++cf) a2[q][cf] = mfma16(wd[4 * i + cf], hh[q][i], a2[q][cf]);
  };
  auto spill = [&](int q) {
#pragma unroll
    for (int cf = 0; cf < 4; ++cf) *(f32x4*)(lds + ((q * 8 + w) * 16 + l15) * RED + cf * 64 + g * 16) = a2[q][cf];
  };
  const rsrc_t ro = mk(out, 2048 * 896);
  auto reduce = [&](int q) {
    f32x2 s = {lo16(rc[q]) + rm[q], hi16(rc[q]) + rm[q]};
#pragma unroll
    for (int ww = 0; ww < 8; ++ww) s += *(const f32x2*)(lds + ((q * 8 + ww) * 16 + er) * RED + (lane & 31) * 8);
    const int v = (int)pk(s[0], s[1]);
    const u32x4 ov = {(uint32_t)v, (uint32_t)__builtin_amdgcn_update_dpp(0, v, 0x55, 0xf, 0xf, true),
                      (uint32_t)__builtin_amdgcn_update_dpp(0, v, 0xaa, 0xf, 0xf, true),
                      (uint32_t)__builtin_amdgcn_update_dpp(0, v, 0xff, 0xf, 0xf, true)};
    if ((lane & 3) == 0) BST(ov, ro, er * (128 * 896) + ec * 2, (8 * mt + 2 * q + rp) * 896, AUX_L);
  };
  asm volatile("s_waitcnt vmcnt(9) lgkmcnt(0)" : "+v"(rc[0]), "+v"(rc[1]), "+v"(rc[2]), "+v"(rc[3])::"memory");
#pragma unroll
  for (int i = 0; i < 28; ++i) TIE(wd[i]);
  ST(9);
  gemm2(0);
  SCHED();
  fetch(2);
  VMW(LATE_END < 42 ? 7 : 9);
  gemm2(1);
  SCHED();
  ST(10);
  __builtin_amdgcn_s_barrier();
  fetch(3);
  spill(0); spill(1);
  SCHED();
  ST(11);
  VMW(7);
  gemm2(2);
  spill(2);
  asm volatile("s_waitcnt lgkmcnt(0)" ::: "memory");
  __builtin_amdgcn_s_barrier();
  ST(12);
  reduce(0); reduce(1); reduce(2);
  SCHED();
  VMW(3);
  ST(13);
  gemm2(3);
  spill(3);
  asm volatile("s_waitcnt lgkmcnt(0)" ::: "memory");
  __builtin_amdgcn_s_barrier();
  reduce(3);
  ST_END(ws, 3);
}

extern "C" void kernel_launch(void* const* d_in, const int* in_sizes, int n_in, void* d_out, int out_size,
                              void* d_ws, size_t ws_size, hipStream_t stream) {
  const uint16_t* x = (const uint16_t*)d_in[0];
  const uint16_t* wr = (const uint16_t*)d_in[1];
  const uint16_t* wi = (const uint16_t*)d_in[2];
  const uint16_t* wg = (const uint16_t*)d_in[4];
  const uint16_t* wu = (const uint16_t*)d_in[5];
  const uint16_t* wd = (const uint16_t*)d_in[6];
  char* ws = (char*)d_ws;
  k1<<<128 + 128 + 56, 512, 0, stream>>>(x, wr, wi, wd, ws);
  k2<<<240 + 196, 256, 0, stream>>>(wg, wu, ws);
  k3<<<224, 512, 0, stream>>>(ws, (uint16_t*)d_out);
}
```

```cpp
#include <hip/hip_runtime.h>
#include <stdint.h>
#include <type_traits>

typedef __attribute__((ext_vector_type(8))) __bf16 bf16x8;
typedef __attribute__((ext_vector_type(2))) __bf16 bf16x2;
typedef __attribute__((ext_vector_type(2))) float f32x2;
typedef __attribute__((ext_vector_type(4))) float f32x4;
typedef __attribute__((ext_vector_type(16))) float f32x16;
typedef __attribute__((ext_vector_type(4))) uint32_t u32x4;
typedef __attribute__((ext_vector_type(2))) uint32_t u32x2;
typedef __attribute__((ext_vector_type(4))) short s16x4;
typedef __amdgpu_buffer_rsrc_t rsrc_t;
#define LDSP __attribute__((address_space(3)))
#define DI __device__ __forceinline__
#define SCHED() __builtin_amdgcn_sched_barrier(0)
#define TIE(x) asm volatile("" : "+v"(x))

#define AUX_X 16
#define AUX_L 16

#define ST(i)
#define STC(i)
#define ST_END(ws, kid)
#define ST_BEGIN

DI f32x4 mfma16(u32x4 a, u32x4 b, f32x4 c) {
  return __builtin_amdgcn_mfma_f32_16x16x32_bf16(__builtin_bit_cast(bf16x8, a), __builtin_bit_cast(bf16x8, b), c, 0, 0, 0);
}
DI f32x16 mfma32(u32x4 a, u32x4 b, f32x16 c) {
  return __builtin_amdgcn_mfma_f32_32x32x16_bf16(__builtin_bit_cast(bf16x8, a), __builtin_bit_cast(bf16x8, b), c, 0, 0, 0);
}
DI uint32_t pk(float a, float b) {
  f32x2 v = {a, b};
  return __builtin_bit_cast(uint32_t, __builtin_convertvector(v, bf16x2));
}
DI float lo16(uint32_t v) { return __builtin_bit_cast(float, v << 16); }
DI float hi16(uint32_t v) { return __builtin_bit_cast(float, v & 0xffff0000u); }
DI float cosr(float r) { return __builtin_amdgcn_cosf(r); }
DI float sinr(float r) { return __builtin_amdgcn_sinf(r); }
template <int N> DI float ror16(float v) {
  return __builtin_bit_cast(float, __builtin_amdgcn_update_dpp(0, __builtin_bit_cast(int, v), 0x120 + N, 0xf, 0xf, true));
}
DI float sum32(float v) {
  v += ror16<8>(v); v += ror16<4>(v); v += ror16<2>(v); v += ror16<1>(v);
  const uint32_t u = __builtin_bit_cast(uint32_t, v);
  auto s = __builtin_amdgcn_permlane16_swap(u, u, false, false);
  return __builtin_bit_cast(float, (uint32_t)s[0]) + __builtin_bit_cast(float, (uint32_t)s[1]);
}
DI rsrc_t mk(const void* p, uint32_t n) { return __builtin_amdgcn_make_buffer_rsrc((void*)p, 0, n, 0x00020000); }
DI u32x4 bld(rsrc_t r, uint32_t voff, uint32_t soff) { return __builtin_amdgcn_raw_buffer_load_b128(r, voff, soff, 0); }
#define BST(v, r, voff, soff, aux) do { const u32x4 bv_ = (v); __builtin_amdgcn_raw_buffer_store_b128(bv_, r, voff, soff, aux); asm volatile("s_nop 1" ::"v"(bv_) : "memory"); } while (0)
#define BLDS(r, ldsptr, voff, soff) __builtin_amdgcn_raw_ptr_buffer_load_lds(r, (LDSP void*)(ldsptr), 16, voff, soff, 0, 0)
DI u32x4 trpair(const char* p0, const char* p1) {
  s16x4 a0 = __builtin_amdgcn_ds_read_tr16_b64_v4i16((LDSP s16x4*)p0);
  s16x4 a1 = __builtin_amdgcn_ds_read_tr16_b64_v4i16((LDSP s16x4*)p1);
  u32x2 b0 = __builtin_bit_cast(u32x2, a0), b1 = __builtin_bit_cast(u32x2, a1);
  return u32x4{b0[0], b0[1], b1[0], b1[1]};
}

template <int I, int N, class Fn> DI void sfor(Fn&& f) {
  if constexpr (I < N) { f(std::integral_constant<int, I>{}); sfor<I + 1, N>(f); }
}

constexpr int D = 448, F = 1792;
constexpr uint32_t ZS = 28800;
constexpr uint32_t UQ = 28672;
constexpr uint32_t OFF_Z = 0;
constexpr uint32_t OFF_U = 4u << 20;
constexpr uint32_t OFF_MX = 7u << 20;
constexpr uint32_t OFF_FL = OFF_MX + 65536;
constexpr uint32_t OFF_WGU = 8u << 20;
constexpr uint32_t OFF_WD = 12u << 20;
constexpr uint32_t OFF_E = OFF_WD + 7 * 56 * 4096;
constexpr uint32_t OFF_HH = 14u << 20;
constexpr uint32_t OFF_WF = 22u << 20;
constexpr uint32_t OFF_R = 24u << 20;
constexpr uint32_t WS_BYTES = 28u << 20;
constexpr uint32_t NEG2 = 0x80008000u;
constexpr uint32_t SENT = 0xffffffffu;
constexpr float BETA = 0.06681531047810609f;
constexpr float RSQ2 = 0.7071067811865476f;
constexpr int GS = 2064;
#define ST_GRIDS {312, 336, 256}
#define ST_SPLITS {{128, 256, 0}, {224, 0, 0}, {224, 0, 0}}

DI void dft28(int g, int l15, u32x4& Ec, u32x4& Es) {
  const float kf = (float)l15 * (1.f / 28.f);
  float cc[8], ss[8];
#pragma unroll
  for (int j = 0; j < 8; ++j) {
    const float a = (float)(4 * g + (j & 3) + 4 * (j & 4)) * kf;
    cc[j] = cosr(a); ss[j] = sinr(-a);
  }
#pragma unroll
  for (int q = 0; q < 4; ++q) { Ec[q] = pk(cc[2 * q], cc[2 * q + 1]); Es[q] = pk(ss[2 * q], ss[2 * q + 1]); }
  const uint32_t m3 = g == 3 ? 0u : ~0u;
  Ec[2] &= m3; Ec[3] &= m3; Es[2] &= m3; Es[3] &= m3;
}
template <int STEP> DI void dft16(int d0, int l15, float (&tc)[4], float (&ts)[4], u32x4& Tr, u32x4& Ti) {
  float c[4], s[4];
#pragma unroll
  for (int j = 0; j < 4; ++j) {
    const float a = (float)(d0 + STEP * j) * (float)l15;
    tc[j] = cosr(a * (1.f / 448.f)); ts[j] = sinr(a * (1.f / 448.f));
    c[j] = BETA * cosr(a * (1.f / 16.f)); s[j] = BETA * sinr(a * (1.f / 16.f));
  }
  Tr[0] = pk(c[0], c[1]); Tr[1] = pk(c[2], c[3]); Tr[2] = pk(s[0], s[1]); Tr[3] = pk(s[2], s[3]);
  Ti[0] = Tr[2] ^ NEG2; Ti[1] = Tr[3] ^ NEG2; Ti[2] = Tr[0]; Ti[3] = Tr[1];
}
DI int slot_group(int g, int l15) {
  return (l15 == 15 || (l15 == 14 && g >= 2) || (l15 == 0 && g < 2)) ? 14 : (l15 == 14 ? 0 : l15);
}

__global__ __launch_bounds__(512, 2) void k1(const uint16_t* __restrict__ x, const uint16_t* __restrict__ wr,
                                             const uint16_t* __restrict__ wi, const uint16_t* __restrict__ wd,
                                             char* __restrict__ ws) {
  constexpr int RS = 912;
  __shared__ __attribute__((aligned(16))) char lds[16 * RS + 15 * GS];
  ST_BEGIN;
  const int t = threadIdx.x, lane = t & 63, l15 = lane & 15, g = lane >> 4;
  const int w = __builtin_amdgcn_readfirstlane(t >> 6);
  const rsrc_t rws = mk(ws, WS_BYTES);
  if (blockIdx.x >= 256) {
    const int kk = blockIdx.x - 256;
    const rsrc_t rd = mk(wd, D * F * 2);
#pragma unroll
    for (int i = 0; i < 4; ++i)
      if (i < 3 || w < 4) BLDS(rd, lds + (i * 8 + w) * 1024, lane * 16, kk * 28672 + (i * 8 + w) * 1024);
    if (t < 8) __builtin_amdgcn_raw_buffer_store_b32(0u, rws, t * 128, OFF_FL + kk * 1024, AUX_X);
    if (w < 7) {
      const int id = kk * 7 + w, ks = id % 28, d = 32 * (id / 28) + (lane & 31);
      const int kd1 = ks >= 2 ? ks >> 1 : (ks ? 0 : 14), e = 8 * (ks & 1) + 4 * (lane >> 5);
      const float a0 = (float)(kd1 * d) * (1.f / 448.f);
      u32x4 o;
#pragma unroll
      for (int r = 0; r < 4; ++r) {
        const float a = a0 + (float)(((e + r) * d) & 15) * (1.f / 16.f);
        const float sc = (r == 0 && ks == 1 && lane < 32) ? BETA * RSQ2 : BETA;
        o[r] = pk(sc * cosr(a), -sc * sinr(a));
      }
      BST(o, rws, lane * 16, OFF_E + id * 1024, AUX_X);
    }
    __syncthreads();
    const int q = (lane >> 2) & 3, p = lane & 3;
    const char* b = lds + (16 * (g >> 1) + 4 * (g & 1) + q) * 896 + p * 8;
#pragma unroll
    for (int i = 0; i < 4; ++i) {
      const int cf = w + 8 * i;
      if (cf < 28) {
        const u32x4 o = trpair(b + cf * 32, b + cf * 32 + 8 * 896);
        BST(o, rws, lane * 16, OFF_WD + (((cf >> 2) * 56 + kk) * 4 + (cf & 3)) * 1024, AUX_X);
      }
    }
    ST_END(ws, 1);
    return;
  }
  if (blockIdx.x >= 128) {
    const int u = blockIdx.x - 128, kq = u & 3, gu = (u >> 2) & 3, tt = (u >> 4) & 1, rho = u >> 5;
    const rsrc_t rr = mk(wr, 2048 * 225 * 2), ri = mk(wi, 2048 * 225 * 2);
    {
      const int n = 2 * w + (lane >> 5);
      const int k = 4 * kq + (n & 3) + 16 * rho + 64 * (16 * tt + 4 * gu + (n >> 2));
      const uint32_t o0 = ((uint32_t)(k * 450) & ~15u) + (lane & 31) * 16;
      const uint32_t o1 = ((uint32_t)(((2048 - k) & 2047) * 450) & ~15u) + (lane & 31) * 16;
      BLDS(rr, lds + w * 1024, o0, 0);
      BLDS(rr, lds + 8192 + w * 1024, o1, 0);
      BLDS(ri, lds + 16384 + w * 1024, o0, 0);
      BLDS(ri, lds + 24576 + w * 1024, o1, 0);
    }
    const int i = lane & 3, e = lane >> 2;
    const int a0 = ((4 * kq + i) * 450) & 15, a1 = (16 - a0) & 15;
    __syncthreads();
#pragma unroll
    for (int n = 0; n < 2; ++n) {
      const int G = w + 8 * n;
      if (G < 14) {
        const int kd = (G ? G : (e < 8 ? 14 : 0)) + 28 * e;
        const bool mir = kd > 224;
        const char* p = lds + (mir ? 8192 + a1 + 2 * (448 - kd) : a0 + 2 * kd) + i * 512;
        const uint32_t sg = mir ? 0x80000000u : 0u;
        u32x4 o;
#pragma unroll
        for (int r = 0; r < 4; ++r) {
          const uint32_t re = *(const uint16_t*)(p + r * 2048), im = *(const uint16_t*)(p + 16384 + r * 2048);
          o[r] = (re | (im << 16)) ^ sg;
        }
        BST(o, rws, e * 8192 + i * 16, OFF_WF + G * (16 * 8192) + (rho * 2 + tt) * 1024 + (16 * gu + 4 * kq) * 16, AUX_X);
      }
    }
    ST_END(ws, 1);
    return;
  }
  const int s2 = blockIdx.x;
  const int s1 = t >> 5, c = t & 31;
  const bool ok1 = c < 24;
  const rsrc_t rx = mk(x, 2048 * 896);
  const u32x4 v0 = bld(rx, s1 * (128 * 896) + c * 16, s2 * 896);
  const u32x4 v1 = bld(rx, s1 * (128 * 896) + (ok1 ? c + 32 : c) * 16, s2 * 896);
  SCHED();
  u32x4 Ec, Es, Tr, Ti;
  float tc[4], ts[4];
  dft28(g, l15, Ec, Es);
  dft16<1>(4 * g, l15, tc, ts, Tr, Ti);
  uint32_t b0 = 2 * w * RS + lane * 8, b1 = b0 + ((g == 3) ? 0 : 512);
  uint32_t wa0 = s1 * RS + c * 16, wa1 = s1 * RS + (ok1 ? c * 16 + 512 : 896);
  uint32_t zw = 16 * RS + slot_group(g, l15) * GS + g * 512 + w * 16;
  uint32_t zr_ = 16 * RS + (w >> 1) * GS + (w & 1) * 1024 + (lane >> 3) * 128 + (lane & 3) * 32 + ((lane >> 2) & 1) * 8;
  uint32_t mo = c == 0 ? s1 * 4 : 0x80000000u;
  float k1m = ok1 ? 1.f : 0.f, m28 = l15 == 0 ? -28.f : 0.f, nyq = lane == 32 ? RSQ2 : 1.f;
#pragma unroll
  for (int i = 0; i < 4; ++i) { TIE(tc[i]); TIE(ts[i]); }
  TIE(Ec); TIE(Es); TIE(Tr); TIE(Ti); TIE(b0); TIE(b1); TIE(wa0); TIE(wa1); TIE(zw); TIE(zr_); TIE(mo); TIE(k1m); TIE(m28); TIE(nyq);
  SCHED();
  *(u32x4*)(lds + wa0) = v0;
  *(u32x4*)(lds + wa1) = v1;
  ST(1);
  const f32x4 zero = {0.f, 0.f, 0.f, 0.f};
  f32x4 xr[2], xi[2];
#pragma unroll
  for (int n = 0; n < 2; ++n) {
    const u32x4 a = trpair(lds + b0 + n * RS, lds + b1 + n * RS);
    xr[n] = mfma16(a, Ec, zero);
    xi[n] = mfma16(a, Es, zero);
  }
  float mean, rstd;
  {
    const bf16x2 one = {(__bf16)1.f, (__bf16)1.f};
    float a0 = 0.f, a1 = 0.f, q0 = 0.f, q1 = 0.f;
#pragma unroll
    for (int i = 0; i < 4; ++i) {
      const uint32_t u0 = v0[i], u1 = v1[i];
      const bf16x2 e0 = __builtin_bit_cast(bf16x2, u0), e1 = __builtin_bit_cast(bf16x2, u1);
      a0 = __builtin_amdgcn_fdot2_f32_bf16(e0, one, a0, false); a1 = __builtin_amdgcn_fdot2_f32_bf16(e1, one, a1, false);
      q0 = __builtin_amdgcn_fdot2_f32_bf16(e0, e0, q0, false); q1 = __builtin_amdgcn_fdot2_f32_bf16(e1, e1, q1, false);
    }
    mean = sum32(__builtin_fmaf(a1, k1m, a0)) * (1.f / D);
    const float var = __builtin_fmaxf(sum32(__builtin_fmaf(q1, k1m, q0)) * (1.f / D) - mean * mean, 0.f);
    rstd = __builtin_amdgcn_rsqf(var + 1e-5f);
    __builtin_amdgcn_raw_buffer_store_b32(__builtin_bit_cast(uint32_t, mean), rws, mo, OFF_MX + s2 * 64, AUX_X);
  }
  f32x4 zr[2], zi[2];
  float rs[2];
#pragma unroll
  for (int n = 0; n < 2; ++n) {
    const float mn = __builtin_bit_cast(float, __builtin_amdgcn_readlane(__builtin_bit_cast(int, mean), 32 * n)) * m28;
    rs[n] = __builtin_bit_cast(float, __builtin_amdgcn_readlane(__builtin_bit_cast(int, rstd), 32 * n));
    float tr[4], ti[4];
#pragma unroll
    for (int r = 0; r < 4; ++r) {
      tr[r] = __builtin_fmaf(xi[n][r], ts[r], __builtin_fmaf(xr[n][r], tc[r], mn));
      ti[r] = __builtin_fmaf(xi[n][r], tc[r], -(xr[n][r] * ts[r]));
    }
    const u32x4 b = {pk(tr[0], tr[1]), pk(tr[2], tr[3]), pk(ti[0], ti[1]), pk(ti[2], ti[3])};
    zr[n] = mfma16(Tr, b, zero);
    zi[n] = mfma16(Ti, b, zero);
  }
  zr[0][0] *= nyq; zr[1][0] *= nyq;
#pragma unroll
  for (int r = 0; r < 4; ++r)
    *(u32x4*)(lds + zw + r * 128) = u32x4{pk(zr[0][r] * rs[0], zi[0][r] * rs[0]), pk(zr[1][r] * rs[1], zi[1][r] * rs[1]),
                                          pk(zr[0][r], zi[0][r]), pk(zr[1][r], zi[1][r])};
  ST(2);
  __syncthreads();
  ST(3);
#pragma unroll
  for (int i = 0; i < 4; ++i)
    if (i < 3 || w < 4) {
      const u32x2 o0 = *(const u32x2*)(lds + zr_ + i * (4 * GS)), o1 = *(const u32x2*)(lds + zr_ + i * (4 * GS) + 16);
      BST((u32x4{o0[0], o0[1], o1[0], o1[1]}), rws, lane * 16, OFF_Z + s2 * ZS + (i * 8 + w) * 1024, AUX_X);
    }
  ST_END(ws, 1);
}

template <int N> DI void rot_acc(f32x4& tr, f32x4& ti, u32x4 d, u32x4 P, u32x4 Q, u32x4 nP, u32x4 nQ) {
  const u32x4 cr = (N == 0) ? P : (N == 1) ? Q : (N == 2) ? nP : nQ;
  const u32x4 ci = (N == 0) ? Q : (N == 1) ? nP : (N == 2) ? nQ : P;
  tr = mfma16(cr, d, tr); ti = mfma16(ci, d, ti);
}
template <int KS> DI void k2_inv(f32x4 (&ur)[2], f32x4 (&ui)[2], const u32x4 (&rf)[8], u32x4 P, u32x4 Q) {
  const u32x4 nP = P ^ NEG2, nQ = Q ^ NEG2;
#pragma unroll
  for (int tb = 0; tb < 2; ++tb) rot_acc<0>(ur[tb], ui[tb], rf[tb], P, Q, nP, nQ);
#pragma unroll
  for (int tb = 0; tb < 2; ++tb) rot_acc<(4 - (KS & 3)) & 3>(ur[tb], ui[tb], rf[2 + tb], P, Q, nP, nQ);
#pragma unroll
  for (int tb = 0; tb < 2; ++tb) rot_acc<(4 - ((2 * KS) & 3)) & 3>(ur[tb], ui[tb], rf[4 + tb], P, Q, nP, nQ);
#pragma unroll
  for (int tb = 0; tb < 2; ++tb) rot_acc<(4 - ((3 * KS) & 3)) & 3>(ur[tb], ui[tb], rf[6 + tb], P, Q, nP, nQ);
}

__global__ __launch_bounds__(256, 2) void k2(const uint16_t* __restrict__ wg, const uint16_t* __restrict__ wu,
                                             char* __restrict__ ws) {
  __shared__ __attribute__((aligned(16))) char lds[15 * GS];
  ST_BEGIN;
  const int t = threadIdx.x, lane = t & 63, l15 = lane & 15, g = lane >> 4;
  const int w = __builtin_amdgcn_readfirstlane(t >> 6);
  const rsrc_t rws = mk(ws, WS_BYTES);
  const f32x4 zero = {0.f, 0.f, 0.f, 0.f};
  if (blockIdx.x >= 224) {
    const int u = blockIdx.x - 224, mat = u & 1, fb = u >> 1;
    const rsrc_t rw = mk(mat ? wu : wg, D * F * 2);
    const uint32_t vo = (lane >> 2) * (F * 2) + (((lane & 3) ^ w) << 4);
#pragma unroll
    for (int i = 0; i < 7; ++i) BLDS(rw, lds + (w + 4 * i) * 1024, vo, fb * 64 + (w + 4 * i) * (16 * F * 2));
    u32x4 Ec, Es, Tr, Ti;
    float tc[4], ts[4];
    dft28(g, l15, Ec, Es);
    dft16<4>(g, l15, tc, ts, Tr, Ti);
    const int q = (lane >> 2) & 3, p = lane & 3;
    uint32_t b0 = (4 * g + q) * 1024 + p * 64 + ((w ^ q) << 4), b1 = b0 + (g == 3 ? 0 : 16384);
    uint32_t zw = slot_group(g, l15) * GS + g * 512 + w * 128;
    float nyq = lane == 32 ? RSQ2 : 1.f;
#pragma unroll
    for (int i = 0; i < 4; ++i) { TIE(tc[i]); TIE(ts[i]); }
    TIE(Ec); TIE(Es); TIE(Tr); TIE(Ti); TIE(b0); TIE(b1); TIE(zw); TIE(nyq);
    __syncthreads();
    u32x4 a[2][4];
#pragma unroll
    for (int i = 0; i < 2; ++i)
#pragma unroll
      for (int tt = 0; tt < 4; ++tt) a[i][tt] = trpair(lds + b0 + tt * 256 + i * 8, lds + b1 + tt * 256 + i * 8);
    __syncthreads();
#pragma unroll
    for (int i = 0; i < 2; ++i) {
      f32x4 xr[4], xi[4];
#pragma unroll
      for (int tt = 0; tt < 4; ++tt) { xr[tt] = mfma16(a[i][tt], Ec, zero); xi[tt] = mfma16(a[i][tt], Es, zero); }
#pragma unroll
      for (int r = 0; r < 4; ++r) {
        float tr[4], ti[4];
#pragma unroll
        for (int tt = 0; tt < 4; ++tt) {
          tr[tt] = __builtin_fmaf(xi[tt][r], ts[tt], xr[tt][r] * tc[tt]);
          ti[tt] = __builtin_fmaf(xi[tt][r], tc[tt], -(xr[tt][r] * ts[tt]));
        }
        const u32x4 b = {pk(tr[0], tr[1]), pk(tr[2], tr[3]), pk(ti[0], ti[1]), pk(ti[2], ti[3])};
        const f32x4 zr = mfma16(Tr, b, zero), zi = mfma16(Ti, b, zero);
        *(u32x4*)(lds + zw + (4 * i + r) * 16) = u32x4{pk(zr[0] * nyq, zi[0]), pk(zr[1], zi[1]), pk(zr[2], zi[2]), pk(zr[3], zi[3])};
      }
    }
    __syncthreads();
    const uint32_t so = ((lane >> 4) & 1) * 28672 + (lane >> 5) * 512 + l15 * 16;
#pragma unroll
    for (int i = 0; i < 7; ++i) {
      const int ks = w + 4 * i;
      const u32x4 o = *(const u32x4*)(lds + (ks >> 1) * GS + (ks & 1) * 1024 + lane * 16);
      BST(o, rws, so, OFF_WGU + fb * 57344 + mat * 256 + ks * 1024, AUX_X);
    }
    ST_END(ws, 2);
    return;
  }
  const int slot = (blockIdx.x & 7) * 28 + (blockIdx.x >> 3);
  u32x4 zf[8], wf[2], zx[2];
  {
    const uint32_t so = OFF_Z + slot * 128, vz = l15 * ZS + (g << 4);
#pragma unroll
    for (int i = 0; i < 8; ++i) zf[i] = bld(rws, vz, so + i * (16 * ZS));
    wf[0] = bld(rws, lane * 16, OFF_WF + (slot * 4 + w) * 2048);
    wf[1] = bld(rws, lane * 16, OFF_WF + (slot * 4 + w) * 2048 + 1024);
    zx[0] = bld(rws, vz, so + 64 + (2 * w) * (16 * ZS));
    zx[1] = bld(rws, vz, so + 64 + (2 * w + 1) * (16 * ZS));
  }
  SCHED();
  u32x4 Br[4], Bi[4], Ar, Ai, C1[2], S1[2];
  float fc[8], fs[8], ic[8], is[8];
  {
    const float lf = (float)l15, g4 = (float)(4 * g), wf_ = (float)w;
    const float kc = lf + 16.f * wf_;
#pragma unroll
    for (int a = 0; a < 4; ++a)
#pragma unroll
      for (int q = 0; q < 4; ++q) {
        const float an = (4.f * (g4 + q) + a) * (kc * (1.f / 64.f));
        const float c = cosr(an), s = sinr(an);
        Br[a][q] = pk(c, s); Bi[a][q] = pk(-s, c);
      }
#pragma unroll
    for (int j = 0; j < 8; ++j) {
      const float an = (g4 + (float)((j & 3) + 4 * (j & 4))) * (kc * (1.f / 2048.f));
      fc[j] = cosr(an); fs[j] = sinr(an);
    }
    const uint32_t lsg = (l15 & 1) ? NEG2 : 0u;
#pragma unroll
    for (int q = 0; q < 2; ++q) {
      const float a0 = (g4 + 2 * q) * (lf * (1.f / 32.f)), a1 = a0 + lf * (1.f / 32.f);
      C1[0][q] = pk(cosr(a0), cosr(a1)); S1[0][q] = pk(sinr(a0), sinr(a1));
      C1[0][q + 2] = C1[0][q] ^ lsg; S1[0][q + 2] = S1[0][q] ^ lsg;
    }
    C1[1] = C1[0] ^ 0x80000000u; S1[1] = S1[0] ^ 0x80000000u;
#pragma unroll
    for (int j = 0; j < 8; ++j) {
      const float an = (g4 + (float)(j & 3) + 16.f * wf_) * ((lf + (float)(4 * (j & 4))) * (1.f / 2048.f));
      ic[j] = cosr(an) * (1.f / 2048.f); is[j] = sinr(an) * (1.f / 2048.f);
    }
    float c4[4], s4[4];
#pragma unroll
    for (int q = 0; q < 4; ++q) {
      const float a = (g4 + q) * ((4.f * lf + wf_) * (1.f / 64.f));
      c4[q] = cosr(a); s4[q] = sinr(a);
    }
    Ar[0] = pk(c4[0], c4[1]); Ar[1] = pk(c4[2], c4[3]);
    Ai[0] = pk(s4[0], s4[1]); Ai[1] = pk(s4[2], s4[3]);
    Ar[2] = Ai[0] ^ NEG2; Ar[3] = Ai[1] ^ NEG2; Ai[2] = Ar[0]; Ai[3] = Ar[1];
#pragma unroll
    for (int j = 0; j < 8; ++j) { TIE(fc[j]); TIE(fs[j]); TIE(ic[j]); TIE(is[j]); }
    TIE(Ar); TIE(Ai);
  }
  SCHED();
  ST(3);
  f32x4 tr[2] = {zero, zero}, ti[2] = {zero, zero};
#pragma unroll
  for (int a = 0; a < 4; ++a)
#pragma unroll
    for (int tb = 0; tb < 2; ++tb) { tr[tb] = mfma16(zf[2 * a + tb], Br[a], tr[tb]); ti[tb] = mfma16(zf[2 * a + tb], Bi[a], ti[tb]); }
  {
    float pr[8], pi[8];
#pragma unroll
    for (int j = 0; j < 8; ++j) {
      const float a = tr[j >> 2][j & 3], b = ti[j >> 2][j & 3];
      pr[j] = __builtin_fmaf(b, fs[j], a * fc[j]);
      pi[j] = __builtin_fmaf(b, fc[j], -(a * fs[j]));
    }
    const u32x4 Pr = {pk(pr[0], pr[1]), pk(pr[2], pr[3]), pk(pr[4], pr[5]), pk(pr[6], pr[7])};
    const u32x4 Pi = {pk(pi[0], pi[1]), pk(pi[2], pi[3]), pk(pi[4], pi[5]), pk(pi[6], pi[7])};
    const u32x4 Nr = Pr ^ NEG2;
    float yr[8], yi[8];
#pragma unroll
    for (int tt = 0; tt < 2; ++tt) {
      f32x4 xr = mfma16(C1[tt], Pr, zero), xi = mfma16(C1[tt], Pi, zero);
      xr = mfma16(S1[tt], Pi, xr); xi = mfma16(S1[tt], Nr, xi);
#pragma unroll
      for (int r = 0; r < 4; ++r) {
        const float a = lo16(wf[tt][r]), b = hi16(wf[tt][r]);
        yr[4 * tt + r] = xr[r] * a - xi[r] * b;
        yi[4 * tt + r] = xr[r] * b + xi[r] * a;
      }
    }
    const u32x4 Yr = {pk(yr[0], yr[1]), pk(yr[2], yr[3]), pk(yr[4], yr[5]), pk(yr[6], yr[7])};
    const u32x4 Yi = {pk(yi[0], yi[1]), pk(yi[2], yi[3]), pk(yi[4], yi[5]), pk(yi[6], yi[7])};
    const u32x4 Ni = Yi ^ NEG2;
#pragma unroll
    for (int tb = 0; tb < 2; ++tb) {
      f32x4 rr = mfma16(Yr, C1[tb], zero), ri = mfma16(Yr, S1[tb], zero);
      rr = mfma16(Ni, S1[tb], rr); ri = mfma16(Yi, C1[tb], ri);
      float vr[4], vi[4];
#pragma unroll
      for (int r = 0; r < 4; ++r) {
        vr[r] = __builtin_fmaf(rr[r], ic[4 * tb + r], -(ri[r] * is[4 * tb + r]));
        vi[r] = __builtin_fmaf(ri[r], ic[4 * tb + r], rr[r] * is[4 * tb + r]);
      }
      *(u32x4*)(lds + (w * 2 + tb) * 1024 + lane * 16) = u32x4{pk(vr[0], vr[1]), pk(vr[2], vr[3]), pk(vi[0], vi[1]), pk(vi[2], vi[3])};
    }
  }
  ST(1);
  __syncthreads();
  ST(2);
  u32x4 rf[8];
#pragma unroll
  for (int i = 0; i < 8; ++i) rf[i] = *(const u32x4*)(lds + i * 1024 + lane * 16);
  f32x4 ur[2], ui[2];
#pragma unroll
  for (int tb = 0; tb < 2; ++tb)
#pragma unroll
    for (int r = 0; r < 4; ++r) { ur[tb][r] = lo16(zx[tb][r]); ui[tb][r] = hi16(zx[tb][r]); }
  if (w == 0) k2_inv<0>(ur, ui, rf, Ar, Ai);
  else if (w == 1) k2_inv<1>(ur, ui, rf, Ar, Ai);
  else if (w == 2) k2_inv<2>(ur, ui, rf, Ar, Ai);
  else k2_inv<3>(ur, ui, rf, Ar, Ai);
  const uint32_t im = slot == 8 ? 0u : ~0u;
#pragma unroll
  for (int tb = 0; tb < 2; ++tb) {
    u32x4 o = {pk(ur[tb][0], ur[tb][1]), pk(ur[tb][2], ur[tb][3]), pk(ui[tb][0], ui[tb][1]) & im, pk(ui[tb][2], ui[tb][3]) & im};
    BST(o, rws, (l15 >> 1) * UQ + (l15 & 1) * 64 + g * 16, OFF_U + (16 * w + 8 * tb) * UQ + slot * 128, AUX_X);
  }
  ST_END(ws, 2);
}

#define ALOAD(dst, voff, rs, soff) asm volatile("buffer_load_dwordx4 %0, %1, %2, %3 offen" : "=v"(dst) : "v"(voff), "s"(rs), "s"(soff) : "memory")
#define ALOADC(dst, voff, rs, soff) asm volatile("buffer_load_dwordx4 %0, %1, %2, %3 offen sc1" : "=v"(dst) : "v"(voff), "s"(rs), "s"(soff) : "memory")
#define ALOAD1(dst, voff, rs, soff) asm volatile("buffer_load_dword %0, %1, %2, %3 offen" : "=v"(dst) : "v"(voff), "s"(rs), "s"(soff) : "memory")
#define ALOAD1C(dst, voff, rs, soff) asm volatile("buffer_load_dword %0, %1, %2, %3 offen sc1" : "=v"(dst) : "v"(voff), "s"(rs), "s"(soff) : "memory")
#define ADMA(ldsa, voff, rs, soff) asm volatile("s_mov_b32 m0, %0\n\ts_nop 0\n\tbuffer_load_dwordx4 %1, %2, %3 offen lds" ::"s"(ldsa), "v"(voff), "s"(rs), "s"(soff) : "memory")
#define VMW(N) asm volatile("s_waitcnt vmcnt(%0)" ::"n"(N) : "memory")
#define VMWT(N, x) asm volatile("s_waitcnt vmcnt(%1)" : "+v"(x) : "n"(N) : "memory")
#define BAR() asm volatile("s_barrier" ::: "memory")
#define ARELOADC(dst, voff, rs, soff) asm volatile("buffer_load_dwordx4 %0, %1, %2, %3 offen sc1" : "+v"(dst) : "v"(voff), "s"(rs), "s"(soff) : "memory")
constexpr int RED = 272;
constexpr int L_SMALL = 32 * 16 * RED;
DI uint32_t umax(uint32_t a, uint32_t b) { return a > b ? a : b; }
DI uint32_t umin(uint32_t a, uint32_t b) { return a < b ? a : b; }
DI uint32_t max3u(uint32_t a, uint32_t b, uint32_t c) { return umax(umax(a, b), c); }

#define LAG1 12
#define LAG2 20
constexpr int step_q(int i) { const int u = i % 56, L = i < 56 ? LAG1 : LAG2; return 2 * (i / 56) + (u < L ? 0 : u >= 56 - L ? 1 : ((u - L) & 1) ^ 1); }
constexpr int step_k(int i) { const int u = i % 56, L = i < 56 ? LAG1 : LAG2; return u < L ? u : u >= 56 - L ? u - 28 : ((u - L) & 1) ? L + (u - L) / 2 : (u - L) / 2; }
#define NE 16
#define WE 13
#define R1M 3
#define R2M 2
#define SB3 87
#define PH 99
#define F2 0
#define F3 2
#define WD0 44
#define MK0 40
#define GD2 1
constexpr int SB1 = 24, SB2 = 52, G0 = 57 - LAG1, G2 = 113 - LAG2, V1 = 53;
static_assert(SB3 <= 107 - LAG2 && SB3 > V1 + 32 && LAG1 >= 12 && LAG2 >= 20, "schedule");
constexpr int prio(int i) { return 3 - (i < SB1 ? i * 4 / SB1 : i < SB2 ? (i - SB1) * 4 / (SB2 - SB1) : i < SB3 ? (i - SB2) * 4 / (SB3 - SB2) : (i - SB3) * 4 / (112 - SB3)); }
constexpr int I_DM = 28, I_WD = 35, I_H0 = 63, I_END = 70;
constexpr int I_PB = 90, I_FLAG = 94, I_POLLH = 95, I_MK = 96;
struct Sched { int at[112]; int pos[100]; int cnt[113]; int next; };
constexpr Sched make_sched() {
  Sched s{};
  int n = 0, nx = WE;
  for (int k = 0; k < WE; ++k) s.pos[k] = n++;
  for (int i = 0; i < 112; ++i) {
    s.cnt[i] = n;
    int it = -1, freed = 0;
    for (int j = 0; j <= i - 2; ++j) freed += step_q(j) == 3;
    if (i == SB3) it = I_FLAG;
    else if (i == G0 + 9) it = I_PB;
    else if (i == 75) it = I_PB + 1;
    else if (i == G2 + 18 / GD2) it = I_PB + 2;
    else if (i == PH) it = I_POLLH;
    else if (i == MK0) it = I_MK;
    else if (i == MK0 + 2) it = I_MK + 1;
    else if (nx < I_WD) { if (i % R1M != R1M - 1) it = nx++; }
    else if (nx < I_END && i >= WD0 && (i % R2M != R2M - 1 || nx >= I_H0) && nx - I_WD < NE + freed && (nx != I_H0 || i >= PH + 10)) it = nx++;
    s.at[i] = it;
    if (it >= 0) s.pos[it] = n++;
  }
  s.cnt[112] = n;
  s.next = nx;
  for (int k = nx; k < I_END; ++k) s.pos[k] = n++;
  s.pos[I_END] = n;
  return s;
}
constexpr Sched SC = make_sched();

__global__ __launch_bounds__(512, 2) void k3(char* __restrict__ ws, uint16_t* __restrict__ out) {
  __shared__ __attribute__((aligned(16))) char lds[L_SMALL + 2048];
  ST_BEGIN;
  const int t = threadIdx.x, lane = t & 63, l15 = lane & 15, g = lane >> 4;
  const int w = __builtin_amdgcn_readfirstlane(t >> 6);
  const int xcd = blockIdx.x & 7, jb = blockIdx.x >> 3;
  const int mt = 4 * (xcd >> 1) + (jb & 3), rp = xcd & 1, ct = jb >> 2, nt = 7 * rp + ct;
  const rsrc_t rws = mk(ws, WS_BYTES);
  const uint32_t vo = lane * 16;
  const uint32_t lb = (uint32_t)(uintptr_t)(LDSP char*)lds + w * 7168, sU = OFF_U + mt * (4 * UQ) + w * 7168;
#pragma unroll
  for (int n = 0; n < 7; ++n) ADMA(lb + n * 1024, vo, rws, sU + n * 1024);
  const int ml = lane & 31;
  uint32_t bo[2];
  bo[0] = (lane >> 5) * 512 + ((lane >> 3) & 1) * 128 + (g & 1) * 64 + (lane & 3) * 16 + ((lane >> 2) & 1) * 8;
  bo[1] = bo[0] + 2 * UQ;
  TIE(bo[0]); TIE(bo[1]);
  constexpr int PB = 4;
  if (ct == 7) {
    uint32_t mx[4];
#pragma unroll
    for (int q = 0; q < 4; ++q) ALOAD1(mx[q], ml * 4, rws, OFF_MX + (8 * mt + 2 * q) * 64);
    BAR();
    if (w == 7) {
      VMW(0);
      BAR();
    } else {
      const int j = 7 * rp + w;
      const uint32_t sE = OFF_E + j * (28 * 1024), sR = OFF_R + (mt * 14 + j) * 8192;
      constexpr int EE = 12;
      u32x4 e[28], bf[PB];
#pragma unroll
      for (int i = 0; i < EE; ++i) ALOAD(e[i], vo, rws, sE + i * 1024);
      asm volatile("s_waitcnt vmcnt(%4)" : "+v"(mx[0]), "+v"(mx[1]), "+v"(mx[2]), "+v"(mx[3]) : "n"(EE) : "memory");
      BAR();
      ST(1);
      f32x16 acc[4];
#pragma unroll
      for (int q = 0; q < 4; ++q)
#pragma unroll
        for (int r = 0; r < 16; ++r) acc[q][r] = __builtin_bit_cast(float, mx[q]);
      auto put = [&](int q) {
#pragma unroll
        for (int h = 0; h < 2; ++h)
          BST((u32x4{pk(acc[q][8 * h], acc[q][8 * h + 1]), pk(acc[q][8 * h + 2], acc[q][8 * h + 3]), pk(acc[q][8 * h + 4], acc[q][8 * h + 5]),
                     pk(acc[q][8 * h + 6], acc[q][8 * h + 7])}), rws, vo, sR + (q * 2 + h) * 1024, 16);
      };
#pragma unroll
      for (int i = 0; i < PB - 1; ++i) bf[i] = trpair(lds + bo[0] + (i & 1) * UQ + (i >> 1) * 1024, lds + bo[0] + (i & 1) * UQ + (i >> 1) * 1024 + 256);
      sfor<0, 112>([&](auto I) {
        constexpr int i = I, q = 2 * (i / 56) + (i & 1), ks = (i % 56) >> 1, NL = 28 - EE + 8;
        if constexpr (i == 52) { VMW(0); BAR(); }
        if constexpr (i + PB - 1 < 112) {
          constexpr int n = i + PB - 1, off = (n & 1) * UQ + ((n % 56) >> 1) * 1024;
          bf[n % PB] = trpair(lds + bo[n / 56] + off, lds + bo[n / 56] + off + 256);
        }
        if constexpr (q == 0) VMWT(EE + (i < NL ? i : NL) - ks - 1, e[ks]);
        acc[q] = mfma32(e[ks], bf[i % PB], acc[q]);
        if constexpr (i < 28 - EE) ALOAD(e[EE + i], vo, rws, sE + (EE + i) * 1024);
        else if constexpr (i < NL) ADMA(lb + 2 * UQ + (w + i - 28 + EE) * 1024, vo, rws, sU + 2 * UQ + (w + i - 28 + EE) * 1024);
        if constexpr (i == 60) put(0);
        if constexpr (i == 64) put(1);
        SCHED();
      });
      ST(2);
      put(2); put(3);
      VMW(0);
      if (lane == 0) __builtin_amdgcn_raw_buffer_store_b32(1u, rws, 0, OFF_FL + (224 + mt * 14 + j) * 128, 16);
    }
    ST_END(ws, 3);
    return;
  }
  u32x4 wf[28];
  const uint32_t sW = OFF_WGU + (nt * 8 + w) * (28 * 1024);
  const uint32_t sP = OFF_HH + (mt * 4 * 112 + nt * 8 + w) * 1024;
  uint32_t va = bo[0] + (w & 1) * UQ + (w >> 1) * 7168;
  TIE(va);
  BAR();
#pragma unroll
  for (int i = 0; i < WE - 7; ++i) ALOAD(wf[i], vo, rws, sW + i * 1024);
  ST(1);
  VMW(WE - 7);
  ST(2);
  BAR();
  float sq[2];
  auto dot = [&](uint32_t x, int a, bool first) {
    const bf16x2 e0 = __builtin_bit_cast(bf16x2, x);
    sq[a] = __builtin_amdgcn_fdot2_f32_bf16(e0, e0, first ? 0.f : sq[a], false);
  };
  auto sq_put = [&](int pair) {
    const uint32_t x0 = __builtin_bit_cast(uint32_t, sq[0] + sq[1]);
    auto s = __builtin_amdgcn_permlane32_swap(x0, x0, false, false);
    *(float*)(lds + L_SMALL + pair * 1024 + (w & 1) * 512 + ml * 16 + (w >> 1) * 4) =
        __builtin_bit_cast(float, (uint32_t)s[0]) + __builtin_bit_cast(float, (uint32_t)s[1]);
  };
  {
    u32x4 vf[7];
#pragma unroll
    for (int i = 0; i < 7; ++i) vf[i] = trpair(lds + va + i * 1024, lds + va + i * 1024 + 256);
#pragma unroll
    for (int i = 0; i < 7; ++i) {
      ALOAD(wf[WE - 7 + i], vo, rws, sW + (WE - 7 + i) * 1024);
#pragma unroll
      for (int j = 0; j < 4; ++j) dot(vf[i][j], j & 1, i == 0 && j < 2);
      SCHED();
    }
    sq_put(0);
  }
  __builtin_amdgcn_s_setprio(3);
  SCHED();
  ST(3);

  const uint32_t sD = OFF_WD + (ct * 56 + 7 * w) * 4096;
  const uint32_t sH = OFF_HH + (mt * 4 * 112 + 14 * w) * 1024 + rp * 256, vH = g * 512 + l15 * 16;
  const uint32_t sF = OFF_FL + mt * (14 * 128);
  u32x4 wd[28], hh[4][7];
  uint32_t pf, pr;
  f32x16 acc[2];
  float o[8], rs[2], c1[2], gu;
  f32x4 ps;
  u32x4 bf[PB], vf[2];
  auto glu = [&](int p, int h) {
    const int e = h >> 1;
    if (h & 1) {
      if (e >= 1) { o[e - 1] *= gu; TIE(o[e - 1]); }
      if (e < 8) { o[e] = __builtin_amdgcn_rcpf(1.f + o[e]); TIE(o[e]); }
    } else {
      if (e >= 1) { gu = acc[p][7 + e] * rs[p]; TIE(gu); }
      if (e < 8) { o[e] = __builtin_amdgcn_exp2f(acc[p][e] * c1[p]); TIE(o[e]); }
    }
  };
  auto publish = [&](int q) {
    BST((u32x4{umin(pk(o[0], o[1]), SENT - 1), umin(pk(o[2], o[3]), SENT - 1), umin(pk(o[4], o[5]), SENT - 1), umin(pk(o[6], o[7]), SENT - 1)}),
        rws, vo, sP + q * (112 * 1024), 16);
  };
  auto pollh = [&]() { ALOAD1C(pf, min((14 * w >> 3) + (lane & 3), (14 * w + 13) >> 3) * 128, rws, sF); };
  auto pollr = [&]() { ALOAD1C(pr, (lane & 1) * 128, rws, sF + (224 + 2 * ct) * 128); };
  auto flagwait = [&]() {
    for (int spin = 0; __builtin_amdgcn_ballot_w64(pf == 0) != 0 && spin < (1 << 14); ++spin) {
      __builtin_amdgcn_s_sleep(1);
      pollh();
      VMWT(0, pf);
    }
  };
  auto flag = [&]() {
    __builtin_amdgcn_raw_buffer_store_b32(1u, rws, t == 0 ? 0u : 0x80000000u, sF + nt * 128, 16);
  };
  auto issue = [&](auto IT) {
    constexpr int it = IT;
    if constexpr (it >= 0 && it < I_DM) ALOAD(wf[it], vo, rws, sW + it * 1024);
    if constexpr (it >= I_DM && it < I_WD) ADMA(lb + 2 * UQ + (it - I_DM) * 1024, vo, rws, sU + 2 * UQ + (it - I_DM) * 1024);
    if constexpr (it >= I_WD && it < I_H0) ALOAD(wd[it - I_WD], vo, rws, sD + (it - I_WD) * 1024);
    if constexpr (it >= I_H0 && it < I_END) ALOADC(hh[0][it - I_H0], vH, rws, sH + (it - I_H0) * 2048);
    if constexpr (it >= I_PB && it < I_PB + 4) publish(it - I_PB);
    if constexpr (it == I_FLAG) flag();
    if constexpr (it == I_POLLH) pollh();
    if constexpr (it >= I_MK && it < I_MK + 2) BST((u32x4{SENT, SENT, SENT, SENT}), rws, vo, sP + (2 + it - I_MK) * (112 * 1024), 16);
  };
#pragma unroll
  for (int i = 0; i < PB - 1; ++i) bf[i] = trpair(lds + bo[0] + i * 1024, lds + bo[0] + i * 1024 + 256);
  sfor<0, 112>([&](auto I) {
    constexpr int i = I, u = i % 56, q = step_q(i), ks = step_k(i);
    if constexpr (i == SB2) { ST(4); VMW(SC.cnt[i] - SC.pos[I_WD - 1] - 1); BAR(); ST(5); }
    if constexpr (i == SB1) BAR();
    if constexpr (i == SB3) { VMW(SC.cnt[i] - SC.pos[I_PB + 1] - 1); BAR(); ST(6); }
    if constexpr (i + PB - 1 < 112) {
      constexpr int n = i + PB - 1, off = (step_q(n) & 1) * UQ + step_k(n) * 1024;
      bf[n % PB] = trpair(lds + bo[n / 56] + off, lds + bo[n / 56] + off + 256);
    }
    constexpr int v = i - V1;
    if constexpr (v >= 0 && v < 28 && v % 4 == 0) vf[(v / 4) & 1] = trpair(lds + va + 2 * UQ + v * 256, lds + va + 2 * UQ + v * 256 + 256);
    if constexpr (q == 0) VMWT(SC.cnt[i] - SC.pos[ks] - 1, wf[ks]);
    if constexpr (i > 0 && prio(i) != prio(i - 1)) __builtin_amdgcn_s_setprio(prio(i));
    if constexpr (SC.at[i] == I_H0) { TIE(acc[0]); TIE(acc[1]); VMWT(SC.cnt[i] - SC.pos[I_POLLH] - 1, pf); flagwait(); ST(7); }
    if constexpr (ks == 0) {
      const f32x16 z = {0.f, 0.f, 0.f, 0.f, 0.f, 0.f, 0.f, 0.f, 0.f, 0.f, 0.f, 0.f, 0.f, 0.f, 0.f, 0.f};
      acc[q & 1] = mfma32(wf[ks], bf[i % PB], z);
    } else {
      acc[q & 1] = mfma32(wf[ks], bf[i % PB], acc[q & 1]);
    }
    if constexpr (v >= 3 && v < 31) { dot(vf[((v - 3) / 4) & 1][(v - 3) % 4], v & 1, v < 5); TIE(sq[v & 1]); }
    if constexpr (v == 31) sq_put(1);
    if constexpr (ks == 25) ps = *(const f32x4*)(lds + L_SMALL + q * 512 + ml * 16);
    if constexpr (ks == 27) { rs[q & 1] = __builtin_amdgcn_rsqf(((ps[0] + ps[1]) + (ps[2] + ps[3])) * (1.f / D) + 1e-5f); TIE(rs[q & 1]); }
    if constexpr (ks == 27) { c1[q & 1] = rs[q & 1] * -1.44269504088896f; }
    if constexpr (i >= G0 && i < G0 + 9) { glu(0, 2 * (i - G0)); glu(0, 2 * (i - G0) + 1); }
    if constexpr (i >= 57 && i < 75) glu(1, i - 57);
    if constexpr (i >= G2 && i < G2 + 18 / GD2) {
#pragma unroll
      for (int h = 0; h < GD2; ++h) glu(0, GD2 * (i - G2) + h);
    }
    issue(std::integral_constant<int, SC.at[i]>{});
    SCHED();
  });
  ST(8);
  sfor<SC.next, I_H0>(issue);
  if constexpr (SC.next <= I_H0) { TIE(acc[1]); VMWT(SC.pos[I_H0] - SC.pos[I_POLLH] - 1, pf); flagwait(); ST(7); }
  sfor<(SC.next > I_H0 ? SC.next : I_H0), I_END>(issue);
  auto fetch = [&](int q, int i) { ALOADC(hh[q][i], vH, rws, sH + q * (112 * 1024) + i * 2048); };
  auto check = [&](int q) {
#pragma unroll
    for (int i = 0; i < 7; ++i) TIE(hh[q][i]);
    for (int spin = 0; spin < (1 << 14); ++spin) {
      uint32_t m = 0;
#pragma unroll
      for (int i = 0; i < 7; ++i) m = max3u(max3u(m, hh[q][i][0], hh[q][i][1]), hh[q][i][2], hh[q][i][3]);
      if (__builtin_amdgcn_ballot_w64(m == SENT) == 0) break;
#pragma unroll
      for (int i = 0; i < 7; ++i) ARELOADC(hh[q][i], vH, rws, sH + q * (112 * 1024) + i * 2048);
      VMW(0);
#pragma unroll
      for (int i = 0; i < 7; ++i) TIE(hh[q][i]);
    }
  };
  f32x4 a2[4][4];
  const f32x4 zero = {0.f, 0.f, 0.f, 0.f};
  auto gemm2 = [&](int q) {
#pragma unroll
    for (int i = 0; i < 7; ++i) {
#pragma unroll
      for (int cf = 0; cf < 4; ++cf) a2[q][cf] = mfma16(wd[4 * i + cf], hh[q][i], i ? a2[q][cf] : zero);
      if (q == F2) fetch(2, i);
      if (q == F3) fetch(3, i);
      SCHED();
    }
  };
  auto spill = [&](int q) {
#pragma unroll
    for (int cf = 0; cf < 4; ++cf) *(f32x4*)(lds + ((q * 8 + w) * 16 + l15) * RED + cf * 64 + g * 16) = a2[q][cf];
  };
  const int er = 2 * w + (lane >> 5), ec = 64 * ct + 2 * ml;
  uint32_t rc[4];
  const rsrc_t ro = mk(out, 2048 * 896);
  auto reduce = [&](int q) {
    f32x2 s = {lo16(rc[q]), hi16(rc[q])};
#pragma unroll
    for (int ww = 0; ww < 8; ++ww) s += *(const f32x2*)(lds + ((q * 8 + ww) * 16 + er) * RED + ml * 8);
    const int v = (int)pk(s[0], s[1]);
    const u32x4 ov = {(uint32_t)v, (uint32_t)__builtin_amdgcn_update_dpp(0, v, 0x55, 0xf, 0xf, true),
                      (uint32_t)__builtin_amdgcn_update_dpp(0, v, 0xaa, 0xf, 0xf, true),
                      (uint32_t)__builtin_amdgcn_update_dpp(0, v, 0xff, 0xf, 0xf, true)};
    BST(ov, ro, (lane & 3) == 0 ? er * (128 * 896) + ec * 2 : 0x80000000u, (8 * mt + 2 * q + rp) * 896, AUX_L);
  };
  pollr();
#pragma unroll
  for (int i = 0; i < 9; ++i) {
    glu(1, 2 * i); glu(1, 2 * i + 1);
    if (i < 7) fetch(1, i);
    SCHED();
  }
  publish(3);
  __builtin_amdgcn_s_setprio(0);
  VMWT(8, pr);
#pragma unroll
  for (int i = 0; i < 28; ++i) TIE(wd[i]);
#pragma unroll
  for (int i = 0; i < 7; ++i) TIE(hh[0][i]);
  for (int spin = 0; __builtin_amdgcn_ballot_w64(pr == 0) != 0 && spin < (1 << 14); ++spin) {
    __builtin_amdgcn_s_sleep(1);
    pollr();
    VMWT(0, pr);
  }
  ST(9);
  gemm2(0);
  {
    const uint32_t vR = (ml >> 4) * 8192 + ((ml >> 3) & 1) * 1024 + (((ml >> 1) & 1) * 32 + rp * 16 + er) * 16 + ((ml >> 2) & 1) * 8 + (ml & 1) * 4;
#pragma unroll
    for (int q = 0; q < 4; ++q) ALOAD1C(rc[q], vR, rws, OFF_R + (mt * 14 + 2 * ct) * 8192 + q * 2048);
  }
  BAR();
  spill(0);
  VMW(1 + (F2 == 0 ? 7 : 0) + 4);
#pragma unroll
  for (int i = 0; i < 7; ++i) TIE(hh[1][i]);
  ST(10);
  gemm2(1);
  spill(1);
  SCHED();
  asm volatile("s_waitcnt vmcnt(%4)" : "+v"(rc[0]), "+v"(rc[1]), "+v"(rc[2]), "+v"(rc[3]) : "n"(F3 == 1 && F2 == 0 ? 7 : F3 == 1 ? 1 : 0) : "memory");
  check(2);
  ST(11);
  gemm2(2);
  spill(2);
  asm volatile("s_waitcnt lgkmcnt(0)" ::: "memory");
  BAR();
  ST(12);
  reduce(0); reduce(1); reduce(2);
  SCHED();
  VMW(3);
  check(3);
  ST(13);
  gemm2(3);
  spill(3);
  asm volatile("s_waitcnt lgkmcnt(0)" ::: "memory");
  BAR();
  reduce(3);
  ST_END(ws, 3);
}

extern "C" void kernel_launch(void* const* d_in, const int* in_sizes, int n_in, void* d_out, int out_size,
                              void* d_ws, size_t ws_size, hipStream_t stream) {
  const uint16_t* x = (const uint16_t*)d_in[0];
  const uint16_t* wr = (const uint16_t*)d_in[1];
  const uint16_t* wi = (const uint16_t*)d_in[2];
  const uint16_t* wg = (const uint16_t*)d_in[4];
  const uint16_t* wu = (const uint16_t*)d_in[5];
  const uint16_t* wd = (const uint16_t*)d_in[6];
  char* ws = (char*)d_ws;
  k1<<<312, 512, 0, stream>>>(x, wr, wi, wd, ws);
  k2<<<336, 256, 0, stream>>>(wg, wu, ws);
  k3<<<256, 512, 0, stream>>>(ws, (uint16_t*)d_out);
}
```

```cpp
#include <hip/hip_runtime.h>
#include <stdint.h>
#include <type_traits>

typedef __attribute__((ext_vector_type(8))) __bf16 bf16x8;
typedef __attribute__((ext_vector_type(2))) __bf16 bf16x2;
typedef __attribute__((ext_vector_type(2))) float f32x2;
typedef __attribute__((ext_vector_type(4))) float f32x4;
typedef __attribute__((ext_vector_type(16))) float f32x16;
typedef __attribute__((ext_vector_type(4))) uint32_t u32x4;
typedef __attribute__((ext_vector_type(2))) uint32_t u32x2;
typedef __attribute__((ext_vector_type(4))) short s16x4;
typedef __amdgpu_buffer_rsrc_t rsrc_t;
#define LDSP __attribute__((address_space(3)))
#define DI __device__ __forceinline__
#define SCHED() __builtin_amdgcn_sched_barrier(0)
#define TIE(x) asm volatile("" : "+v"(x))

#define AUX_X 16
#define AUX_L 16

#define ST(i)
#define STF(i)
#define STT(i)
#define STC(i)
#define ST_END(ws, kid)
#define ST_BEGIN

DI f32x4 mfma16(u32x4 a, u32x4 b, f32x4 c) {
  return __builtin_amdgcn_mfma_f32_16x16x32_bf16(__builtin_bit_cast(bf16x8, a), __builtin_bit_cast(bf16x8, b), c, 0, 0, 0);
}
DI f32x16 mfma32(u32x4 a, u32x4 b, f32x16 c) {
  return __builtin_amdgcn_mfma_f32_32x32x16_bf16(__builtin_bit_cast(bf16x8, a), __builtin_bit_cast(bf16x8, b), c, 0, 0, 0);
}
DI uint32_t pk(float a, float b) {
  f32x2 v = {a, b};
  return __builtin_bit_cast(uint32_t, __builtin_convertvector(v, bf16x2));
}
DI float lo16(uint32_t v) { return __builtin_bit_cast(float, v << 16); }
DI float hi16(uint32_t v) { return __builtin_bit_cast(float, v & 0xffff0000u); }
DI float cosr(float r) { return __builtin_amdgcn_cosf(r); }
DI float sinr(float r) { return __builtin_amdgcn_sinf(r); }
template <int N> DI float ror16(float v) {
  return __builtin_bit_cast(float, __builtin_amdgcn_update_dpp(0, __builtin_bit_cast(int, v), 0x120 + N, 0xf, 0xf, true));
}
DI float sum32(float v) {
  v += ror16<8>(v); v += ror16<4>(v); v += ror16<2>(v); v += ror16<1>(v);
  const uint32_t u = __builtin_bit_cast(uint32_t, v);
  auto s = __builtin_amdgcn_permlane16_swap(u, u, false, false);
  return __builtin_bit_cast(float, (uint32_t)s[0]) + __builtin_bit_cast(float, (uint32_t)s[1]);
}
DI rsrc_t mk(const void* p, uint32_t n) { return __builtin_amdgcn_make_buffer_rsrc((void*)p, 0, n, 0x00020000); }
DI u32x4 bld(rsrc_t r, uint32_t voff, uint32_t soff) { return __builtin_amdgcn_raw_buffer_load_b128(r, voff, soff, 0); }
#define BST(v, r, voff, soff, aux) do { const u32x4 bv_ = (v); __builtin_amdgcn_raw_buffer_store_b128(bv_, r, voff, soff, aux); asm volatile("s_nop 1" ::"v"(bv_) : "memory"); } while (0)
#define BLDSA(r, ldsptr, voff, soff, aux) __builtin_amdgcn_raw_ptr_buffer_load_lds(r, (LDSP void*)(ldsptr), 16, voff, soff, 0, aux)
#define BLDS(r, ldsptr, voff, soff) BLDSA(r, ldsptr, voff, soff, 0)
#define NT 13
#define AUX_H 16
DI u32x4 trpair(const char* p0, const char* p1) {
  s16x4 a0 = __builtin_amdgcn_ds_read_tr16_b64_v4i16((LDSP s16x4*)p0);
  s16x4 a1 = __builtin_amdgcn_ds_read_tr16_b64_v4i16((LDSP s16x4*)p1);
  u32x2 b0 = __builtin_bit_cast(u32x2, a0), b1 = __builtin_bit_cast(u32x2, a1);
  return u32x4{b0[0], b0[1], b1[0], b1[1]};
}

template <int I, int N, class Fn> DI void sfor(Fn&& f) {
  if constexpr (I < N) { f(std::integral_constant<int, I>{}); sfor<I + 1, N>(f); }
}

constexpr int D = 448, F = 1792;
constexpr uint32_t ZS = 28800;
constexpr uint32_t UQ = 28672;
constexpr uint32_t OFF_Z = 0;
constexpr uint32_t OFF_U = 26u << 20;
constexpr uint32_t OFF_MX = (8u << 20) - (256u << 10);
constexpr uint32_t OFF_FL = OFF_MX + 65536;
constexpr uint32_t OFF_WGU = 8u << 20;
constexpr uint32_t OFF_WD = 12u << 20;
constexpr uint32_t OFF_E = OFF_WD + 7 * 56 * 4096;
#define OVL 0
constexpr uint32_t OFF_HH = OVL ? 0 : 14u << 20;
constexpr uint32_t OFF_WF = OVL ? 128 * ZS : 22u << 20;
constexpr uint32_t OFF_R = 24u << 20;
constexpr uint32_t WS_BYTES = 28u << 20;
constexpr uint32_t NEG2 = 0x80008000u;
constexpr uint32_t SENT = 0xffffffffu;
constexpr float BETA = 0.06681531047810609f;
constexpr float RSQ2 = 0.7071067811865476f;
constexpr int GS = 2064;
#define ST_GRIDS {256, 252, 256}
#define ST_SPLITS {{128, 0, 0}, {112, 224, 0}, {224, 0, 0}}
#define NPF 0
#define PFM 2
#define VMW(N) asm volatile("s_waitcnt vmcnt(%0)" ::"n"(N) : "memory")
#define BAR() asm volatile("s_barrier" ::: "memory")
#define ALOAD(dst, voff, rs, soff) asm volatile("buffer_load_dwordx4 %0, %1, %2, %3 offen" : "=v"(dst) : "v"(voff), "s"(rs), "s"(soff) : "memory")
#define TOUCH(dst, voff, rs, soff) asm volatile("buffer_load_dword %0, %1, %2, %3 offen" : "=v"(dst) : "v"(voff), "s"(rs), "s"(soff) : "memory")
DI f32x2 cis(float r) { return f32x2{cosr(r), sinr(r)}; }
DI f32x2 cmul(f32x2 a, f32x2 b) { return __builtin_elementwise_fma(f32x2{a[1], a[0]}, f32x2{-b[1], b[1]}, a * b[0]); }

DI void dft28(int g, int l15, u32x4& Ec, u32x4& Es) {
  const float kf = (float)l15 * (1.f / 28.f);
  float cc[8], ss[8];
#pragma unroll
  for (int j = 0; j < 8; ++j) {
    const float a = (float)(4 * g + (j & 3) + 4 * (j & 4)) * kf;
    cc[j] = cosr(a); ss[j] = sinr(-a);
  }
#pragma unroll
  for (int q = 0; q < 4; ++q) { Ec[q] = pk(cc[2 * q], cc[2 * q + 1]); Es[q] = pk(ss[2 * q], ss[2 * q + 1]); }
  const uint32_t m3 = g == 3 ? 0u : ~0u;
  Ec[2] &= m3; Ec[3] &= m3; Es[2] &= m3; Es[3] &= m3;
}
template <int STEP> DI void dft16(int d0, int l15, float (&tc)[4], float (&ts)[4], u32x4& Tr, u32x4& Ti) {
  float c[4], s[4];
#pragma unroll
  for (int j = 0; j < 4; ++j) {
    const float a = (float)(d0 + STEP * j) * (float)l15;
    tc[j] = cosr(a * (1.f / 448.f)); ts[j] = sinr(a * (1.f / 448.f));
    c[j] = BETA * cosr(a * (1.f / 16.f)); s[j] = BETA * sinr(a * (1.f / 16.f));
  }
  Tr[0] = pk(c[0], c[1]); Tr[1] = pk(c[2], c[3]); Tr[2] = pk(s[0], s[1]); Tr[3] = pk(s[2], s[3]);
  Ti[0] = Tr[2] ^ NEG2; Ti[1] = Tr[3] ^ NEG2; Ti[2] = Tr[0]; Ti[3] = Tr[1];
}
DI int slot_group(int g, int l15) {
  return (l15 == 15 || (l15 == 14 && g >= 2) || (l15 == 0 && g < 2)) ? 14 : (l15 == 14 ? 0 : l15);
}

__global__ __launch_bounds__(512, 2) void k1(const uint16_t* __restrict__ x, const uint16_t* __restrict__ wr,
                                             const uint16_t* __restrict__ wi, const uint16_t* __restrict__ wg,
                                             const uint16_t* __restrict__ wu, char* __restrict__ ws) {
  constexpr int RS = 912;
  __shared__ __attribute__((aligned(16))) char lds[16 * RS + 15 * GS];
  ST_BEGIN;
  const int t = threadIdx.x, lane = t & 63, l15 = lane & 15, g = lane >> 4;
  const int w = __builtin_amdgcn_readfirstlane(t >> 6);
  const rsrc_t rws = mk(ws, WS_BYTES);
  if (blockIdx.x >= 128) {
    const int u = blockIdx.x - 128, kq = u & 3, gu = (u >> 2) & 3, tt = (u >> 4) & 1, rho = u >> 5;
    const rsrc_t rr = mk(wr, 2048 * 225 * 2), ri = mk(wi, 2048 * 225 * 2);
    {
      const int n = 2 * w + (lane >> 5);
      const int k = 4 * kq + (n & 3) + 16 * rho + 64 * (16 * tt + 4 * gu + (n >> 2));
      const uint32_t o0 = ((uint32_t)(k * 450) & ~15u) + (lane & 31) * 16;
      const uint32_t o1 = ((uint32_t)(((2048 - k) & 2047) * 450) & ~15u) + (lane & 31) * 16;
      BLDSA(rr, lds + w * 1024, o0, 0, NT & 2);
      BLDSA(rr, lds + 8192 + w * 1024, o1, 0, NT & 2);
      BLDSA(ri, lds + 16384 + w * 1024, o0, 0, NT & 2);
      BLDSA(ri, lds + 24576 + w * 1024, o1, 0, NT & 2);
    }
    const int id = (w & 3) * 128 + u;
    uint32_t pf = 0;
    auto ahead = [&]() { TOUCH(pf, lane * 64, mk(w & 1 ? wu : wg, NPF * 4096), ((w >> 1) * 128 + u) * 4096); };
    if (PFM == 1) ahead();
    u32x4 eo;
    if (w >= 4) {
      const int ks = id % 28, d = 32 * (id / 28) + (lane & 31);
      const int kd1 = ks >= 2 ? ks >> 1 : (ks ? 0 : 14), e = 8 * (ks & 1) + 4 * (lane >> 5);
      const float a0 = (float)(kd1 * d) * (1.f / 448.f);
#pragma unroll
      for (int r = 0; r < 4; ++r) {
        const float a = a0 + (float)(((e + r) * d) & 15) * (1.f / 16.f);
        const float sc = (r == 0 && ks == 1 && lane < 32) ? BETA * RSQ2 : BETA;
        eo[r] = pk(sc * cosr(a), -sc * sinr(a));
      }
      TIE(eo);
    }
    const int i = lane & 3, e = lane >> 2;
    const int a0 = ((4 * kq + i) * 450) & 15, a1 = (16 - a0) & 15;
    ST(1);
    VMW(PFM == 1 ? 1 : 0);
    if (PFM == 2) ahead();
    BAR();
    ST(2);
    if (w >= 4) {
      if (id < 392) BST(eo, rws, lane * 16, OFF_E + id * 1024, AUX_X);
      if (w == 4) BST((u32x4{0u, 0u, 0u, 0u}), rws, lane < 16 && u < 112 ? (lane >> 2) * 128 + (lane & 3) * 16 : 0x80000000u, OFF_FL + u * 512, AUX_X);
    }
#pragma unroll
    for (int n = 0; n < 2; ++n) {
      const int G = w + 8 * n;
      if (G < 14) {
        const int kd = (G ? G : (e < 8 ? 14 : 0)) + 28 * e;
        const bool mir = kd > 224;
        const char* p = lds + (mir ? 8192 + a1 + 2 * (448 - kd) : a0 + 2 * kd) + i * 512;
        const uint32_t sg = mir ? 0x80000000u : 0u;
        u32x4 o;
#pragma unroll
        for (int r = 0; r < 4; ++r) {
          const uint32_t re = *(const uint16_t*)(p + r * 2048), im = *(const uint16_t*)(p + 16384 + r * 2048);
          o[r] = (re | (im << 16)) ^ sg;
        }
        BST(o, rws, e * 8192 + i * 16, OFF_WF + G * (16 * 8192) + (rho * 2 + tt) * 1024 + (16 * gu + 4 * kq) * 16, AUX_X);
      }
    }
    ST(3);
    asm volatile("s_waitcnt vmcnt(0)" ::"v"(pf) : "memory");
    ST_END(ws, 1);
    return;
  }
  const int s2 = blockIdx.x;
  const int s1 = t >> 5, c = t & 31;
  const bool ok1 = c < 24;
  const rsrc_t rx = mk(x, 2048 * 896);
  u32x4 v0, v1;
  v0 = __builtin_amdgcn_raw_buffer_load_b128(rx, s1 * (128 * 896) + c * 16, s2 * 896, (NT & 1) * 2);
  v1 = __builtin_amdgcn_raw_buffer_load_b128(rx, s1 * (128 * 896) + (ok1 ? c + 32 : c) * 16, s2 * 896, (NT & 1) * 2);
  SCHED();
  u32x4 Ec, Es, Tr, Ti;
  float tc[4], ts[4];
  dft28(g, l15, Ec, Es);
  dft16<1>(4 * g, l15, tc, ts, Tr, Ti);
  uint32_t b0 = 2 * w * RS + lane * 8, b1 = b0 + ((g == 3) ? 0 : 512);
  uint32_t wa0 = s1 * RS + c * 16, wa1 = s1 * RS + (ok1 ? c * 16 + 512 : 896);
  uint32_t zw = 16 * RS + slot_group(g, l15) * GS + g * 512 + w * 16;
  uint32_t zr_ = 16 * RS + (w >> 1) * GS + (w & 1) * 1024 + (lane >> 3) * 128 + (lane & 3) * 32 + ((lane >> 2) & 1) * 8;
  uint32_t mo = c == 0 ? s1 * 4 : 0x80000000u;
  float k1m = ok1 ? 1.f : 0.f, m28 = l15 == 0 ? -28.f : 0.f, nyq = lane == 32 ? RSQ2 : 1.f;
#pragma unroll
  for (int i = 0; i < 4; ++i) { TIE(tc[i]); TIE(ts[i]); }
  TIE(Ec); TIE(Es); TIE(Tr); TIE(Ti); TIE(b0); TIE(b1); TIE(wa0); TIE(wa1); TIE(zw); TIE(zr_); TIE(mo); TIE(k1m); TIE(m28); TIE(nyq);
  SCHED();
  *(u32x4*)(lds + wa0) = v0;
  *(u32x4*)(lds + wa1) = v1;
  ST(1);
  const f32x4 zero = {0.f, 0.f, 0.f, 0.f};
  f32x4 xr[2], xi[2];
#pragma unroll
  for (int n = 0; n < 2; ++n) {
    const u32x4 a = trpair(lds + b0 + n * RS, lds + b1 + n * RS);
    xr[n] = mfma16(a, Ec, zero);
    xi[n] = mfma16(a, Es, zero);
  }
  float mean, rstd;
  {
    const bf16x2 one = {(__bf16)1.f, (__bf16)1.f};
    float a0 = 0.f, a1 = 0.f, q0 = 0.f, q1 = 0.f;
#pragma unroll
    for (int i = 0; i < 4; ++i) {
      const uint32_t u0 = v0[i], u1 = v1[i];
      const bf16x2 e0 = __builtin_bit_cast(bf16x2, u0), e1 = __builtin_bit_cast(bf16x2, u1);
      a0 = __builtin_amdgcn_fdot2_f32_bf16(e0, one, a0, false); a1 = __builtin_amdgcn_fdot2_f32_bf16(e1, one, a1, false);
      q0 = __builtin_amdgcn_fdot2_f32_bf16(e0, e0, q0, false); q1 = __builtin_amdgcn_fdot2_f32_bf16(e1, e1, q1, false);
    }
    mean = sum32(__builtin_fmaf(a1, k1m, a0)) * (1.f / D);
    const float var = __builtin_fmaxf(sum32(__builtin_fmaf(q1, k1m, q0)) * (1.f / D) - mean * mean, 0.f);
    rstd = __builtin_amdgcn_rsqf(var + 1e-5f);
    __builtin_amdgcn_raw_buffer_store_b32(__builtin_bit_cast(uint32_t, mean), rws, mo, OFF_MX + s2 * 64, AUX_X);
  }
  f32x4 zr[2], zi[2];
  float rs[2];
#pragma unroll
  for (int n = 0; n < 2; ++n) {
    const float mn = __builtin_bit_cast(float, __builtin_amdgcn_readlane(__builtin_bit_cast(int, mean), 32 * n)) * m28;
    rs[n] = __builtin_bit_cast(float, __builtin_amdgcn_readlane(__builtin_bit_cast(int, rstd), 32 * n));
    float tr[4], ti[4];
#pragma unroll
    for (int r = 0; r < 4; ++r) {
      tr[r] = __builtin_fmaf(xi[n][r], ts[r], __builtin_fmaf(xr[n][r], tc[r], mn));
      ti[r] = __builtin_fmaf(xi[n][r], tc[r], -(xr[n][r] * ts[r]));
    }
    const u32x4 b = {pk(tr[0], tr[1]), pk(tr[2], tr[3]), pk(ti[0], ti[1]), pk(ti[2], ti[3])};
    zr[n] = mfma16(Tr, b, zero);
    zi[n] = mfma16(Ti, b, zero);
  }
  zr[0][0] *= nyq; zr[1][0] *= nyq;
#pragma unroll
  for (int r = 0; r < 4; ++r)
    *(u32x4*)(lds + zw + r * 128) = u32x4{pk(zr[0][r] * rs[0], zi[0][r] * rs[0]), pk(zr[1][r] * rs[1], zi[1][r] * rs[1]),
                                          pk(zr[0][r], zi[0][r]), pk(zr[1][r], zi[1][r])};
  ST(2);
  asm volatile("s_waitcnt lgkmcnt(0)" ::: "memory");
  BAR();
  ST(3);
  u32x2 o0[4], o1[4];
#pragma unroll
  for (int i = 0; i < 4; ++i) {
    const int ii = i < 3 || w < 4 ? i : 2;
    o0[i] = *(const u32x2*)(lds + zr_ + ii * (4 * GS)); o1[i] = *(const u32x2*)(lds + zr_ + ii * (4 * GS) + 16);
  }
#pragma unroll
  for (int i = 0; i < 4; ++i)
    if (i < 3 || w < 4) BST((u32x4{o0[i][0], o0[i][1], o1[i][0], o1[i][1]}), rws, lane * 16, OFF_Z + s2 * ZS + (i * 8 + w) * 1024, AUX_X);
  ST_END(ws, 1);
}

template <int N> DI void rot_acc(f32x4& tr, f32x4& ti, u32x4 d, u32x4 P, u32x4 Q, u32x4 nP, u32x4 nQ) {
  const u32x4 cr = (N == 0) ? P : (N == 1) ? Q : (N == 2) ? nP : nQ;
  const u32x4 ci = (N == 0) ? Q : (N == 1) ? nP : (N == 2) ? nQ : P;
  tr = mfma16(cr, d, tr); ti = mfma16(ci, d, ti);
}
template <int KS> DI void k2_inv(f32x4 (&ur)[2], f32x4 (&ui)[2], const u32x4 (&rf)[8], u32x4 P, u32x4 Q) {
  const u32x4 nP = P ^ NEG2, nQ = Q ^ NEG2;
#pragma unroll
  for (int tb = 0; tb < 2; ++tb) rot_acc<0>(ur[tb], ui[tb], rf[tb], P, Q, nP, nQ);
#pragma unroll
  for (int tb = 0; tb < 2; ++tb) rot_acc<(4 - (KS & 3)) & 3>(ur[tb], ui[tb], rf[2 + tb], P, Q, nP, nQ);
#pragma unroll
  for (int tb = 0; tb < 2; ++tb) rot_acc<(4 - ((2 * KS) & 3)) & 3>(ur[tb], ui[tb], rf[4 + tb], P, Q, nP, nQ);
#pragma unroll
  for (int tb = 0; tb < 2; ++tb) rot_acc<(4 - ((3 * KS) & 3)) & 3>(ur[tb], ui[tb], rf[6 + tb], P, Q, nP, nQ);
}

__global__ __launch_bounds__(512, 2) void k2(const uint16_t* __restrict__ wg, const uint16_t* __restrict__ wu,
                                             const uint16_t* __restrict__ wd, char* __restrict__ ws) {
  __shared__ __attribute__((aligned(16))) char lds[57344];
  ST_BEGIN;
  const int t = threadIdx.x, lane = t & 63, l15 = lane & 15, g = lane >> 4;
  const int w8 = __builtin_amdgcn_readfirstlane(t >> 6), w = w8 & 3, hf = w8 >> 2;
  const rsrc_t rws = mk(ws, WS_BYTES);
  const f32x4 zero = {0.f, 0.f, 0.f, 0.f};
  const int q = (lane >> 2) & 3, p = lane & 3;
  if (blockIdx.x >= 224) {
    const int kb = blockIdx.x - 224;
    const rsrc_t rd = mk(wd, D * F * 2);
#pragma unroll
    for (int i = 0; i < 7; ++i) BLDSA(rd, lds + (i * 8 + w8) * 1024, lane * 16, kb * 57344 + (i * 8 + w8) * 1024, (NT >> 2) & 2);
    const char* b = lds + hf * 28672 + (16 * (g >> 1) + 4 * (g & 1) + q) * 896 + p * 8 + w * 32;
    u32x4 o[7];
    ST(1);
    __syncthreads();
    ST(2);
#pragma unroll
    for (int i = 0; i < 7; ++i) o[i] = trpair(b + i * 128, b + i * 128 + 8 * 896);
#pragma unroll
    for (int i = 0; i < 7; ++i) BST(o[i], rws, lane * 16, OFF_WD + ((i * 56 + 2 * kb + hf) * 4 + w) * 1024, AUX_X);
    ST_END(ws, 2);
    return;
  }
  if (blockIdx.x >= 112) {
    const int u = blockIdx.x - 112, pp = (u >> 4) * 8 + (u & 7), mat = pp & 1, fb = (pp & ~1) + ((u >> 3) & 1);
    const rsrc_t rw = mk(mat ? wu : wg, D * F * 2);
    const uint32_t vo = (lane >> 2) * (F * 2) + (((lane & 3) ^ w) << 4);
#pragma unroll
    for (int i = 0; i < 4; ++i)
      if (i < 3 || w8 < 4) BLDSA(rw, lds + (w8 + 8 * i) * 1024, vo, fb * 64 + (w8 + 8 * i) * (16 * F * 2), (NT >> 1) & 2);
    u32x4 Ec, Es, Tr, Ti;
    float tc[4], ts[4];
    dft28(g, l15, Ec, Es);
    dft16<4>(g, l15, tc, ts, Tr, Ti);
    uint32_t b0 = (4 * g + q) * 1024 + p * 64 + ((w ^ q) << 4) + hf * 8, b1 = b0 + (g == 3 ? 0 : 16384);
    uint32_t zw = slot_group(g, l15) * GS + g * 512 + w * 128 + hf * 64;
    float nyq = lane == 32 ? RSQ2 : 1.f;
#pragma unroll
    for (int i = 0; i < 4; ++i) { TIE(tc[i]); TIE(ts[i]); }
    TIE(Ec); TIE(Es); TIE(Tr); TIE(Ti); TIE(b0); TIE(b1); TIE(zw); TIE(nyq);
    ST(1);
    __syncthreads();
    ST(2);
    u32x4 a[4];
#pragma unroll
    for (int tt = 0; tt < 4; ++tt) a[tt] = trpair(lds + b0 + tt * 256, lds + b1 + tt * 256);
    __syncthreads();
    {
      f32x4 xr[4], xi[4];
#pragma unroll
      for (int tt = 0; tt < 4; ++tt) { xr[tt] = mfma16(a[tt], Ec, zero); xi[tt] = mfma16(a[tt], Es, zero); }
#pragma unroll
      for (int r = 0; r < 4; ++r) {
        float tr[4], ti[4];
#pragma unroll
        for (int tt = 0; tt < 4; ++tt) {
          tr[tt] = __builtin_fmaf(xi[tt][r], ts[tt], xr[tt][r] * tc[tt]);
          ti[tt] = __builtin_fmaf(xi[tt][r], tc[tt], -(xr[tt][r] * ts[tt]));
        }
        const u32x4 b = {pk(tr[0], tr[1]), pk(tr[2], tr[3]), pk(ti[0], ti[1]), pk(ti[2], ti[3])};
        const f32x4 zr = mfma16(Tr, b, zero), zi = mfma16(Ti, b, zero);
        *(u32x4*)(lds + zw + r * 16) = u32x4{pk(zr[0] * nyq, zi[0]), pk(zr[1], zi[1]), pk(zr[2], zi[2]), pk(zr[3], zi[3])};
      }
    }
    ST(3);
    __syncthreads();
    ST(4);
    const uint32_t so = ((lane >> 4) & 1) * 28672 + (lane >> 5) * 512 + l15 * 16;
#pragma unroll
    for (int i = 0; i < 4; ++i) a[i] = *(const u32x4*)(lds + (w8 >> 1) * GS + (w8 & 1) * 1024 + lane * 16 + i * (4 * GS));
#pragma unroll
    for (int i = 0; i < 4; ++i)
      if (i < 3 || w8 < 4) BST(a[i], rws, so, OFF_WGU + fb * 57344 + mat * 256 + (w8 + 8 * i) * 1024, AUX_X);
    ST_END(ws, 2);
    return;
  }
  const int slot = (blockIdx.x & 7) * 28 + 2 * (blockIdx.x >> 3) + hf;
  char* const lh = lds + hf * 24576;
  u32x4 zf[8], wf[2], zx[2];
  {
    const uint32_t so = OFF_Z + slot * 128 + w * (32 * ZS), vz = (lane >> 3) * ZS + (((lane & 7) ^ (lane >> 4)) << 4);
#pragma unroll
    for (int i = 0; i < 4; ++i) BLDS(rws, lh + 8192 + (4 * w + i) * 1024, vz ^ ((i & 1) << 6), so + i * (8 * ZS));
    wf[0] = bld(rws, lane * 16, OFF_WF + (slot * 4 + w) * 2048);
    wf[1] = bld(rws, lane * 16, OFF_WF + (slot * 4 + w) * 2048 + 1024);
  }
  SCHED();
  u32x4 Br[4], Bi[4], Ar, Ai, C1[2], S1[2];
  f32x4 fc[2], fs[2], ic[2], is[2];
  {
    const float lf = (float)l15, g4 = (float)(4 * g), wf_ = (float)w;
    const float kc = lf + 16.f * wf_;
    {
      const f32x2 st = cis(kc * (1.f / 64.f));
      f32x2 z = cis(g4 * (kc * (1.f / 16.f)));
#pragma unroll
      for (int n = 0; n < 16; ++n) {
        Br[n & 3][n >> 2] = pk(z[0], z[1]); Bi[n & 3][n >> 2] = pk(-z[1], z[0]);
        z = cmul(z, st);
      }
    }
    {
      const f32x2 st = cis(kc * (1.f / 2048.f)), jp = cis(kc * (1.f / 128.f));
      f32x2 z = cis(g4 * (kc * (1.f / 2048.f)));
#pragma unroll
      for (int j = 0; j < 4; ++j) {
        const f32x2 y = cmul(z, jp);
        fc[0][j] = z[0]; fs[0][j] = z[1]; fc[1][j] = y[0]; fs[1][j] = y[1];
        z = cmul(z, st);
      }
    }
    const uint32_t lsg = (l15 & 1) ? NEG2 : 0u;
    {
      const f32x2 st = cis(lf * (1.f / 32.f)), z0 = cis(g4 * (lf * (1.f / 32.f))), z1 = cmul(z0, st), z2 = cmul(z1, st), z3 = cmul(z2, st);
      C1[0][0] = pk(z0[0], z1[0]); C1[0][1] = pk(z2[0], z3[0]); S1[0][0] = pk(z0[1], z1[1]); S1[0][1] = pk(z2[1], z3[1]);
#pragma unroll
      for (int q2 = 0; q2 < 2; ++q2) { C1[0][q2 + 2] = C1[0][q2] ^ lsg; S1[0][q2 + 2] = S1[0][q2] ^ lsg; }
    }
    C1[1] = C1[0] ^ 0x80000000u; S1[1] = S1[0] ^ 0x80000000u;
#pragma unroll
    for (int h = 0; h < 2; ++h) {
      const float b = (lf + (float)(16 * h)) * (1.f / 2048.f);
      const f32x2 st = cis(b);
      f32x2 z = cis((g4 + 16.f * wf_) * b) * (1.f / 2048.f);
#pragma unroll
      for (int j = 0; j < 4; ++j) { ic[h][j] = z[0]; is[h][j] = z[1]; z = cmul(z, st); }
    }
    {
      const float b = (4.f * lf + wf_) * (1.f / 64.f);
      const f32x2 st = cis(b), z0 = cis(g4 * b), z1 = cmul(z0, st), z2 = cmul(z1, st), z3 = cmul(z2, st);
      Ar[0] = pk(z0[0], z1[0]); Ar[1] = pk(z2[0], z3[0]);
      Ai[0] = pk(z0[1], z1[1]); Ai[1] = pk(z2[1], z3[1]);
    }
    Ar[2] = Ai[0] ^ NEG2; Ar[3] = Ai[1] ^ NEG2; Ai[2] = Ar[0]; Ai[3] = Ar[1];
#pragma unroll
    for (int j = 0; j < 2; ++j) { TIE(fc[j]); TIE(fs[j]); TIE(ic[j]); TIE(is[j]); }
    TIE(Ar); TIE(Ai);
  }
  uint32_t za = hf * 24576 + 8192 + l15 * 128 + ((g ^ (l15 >> 1)) << 4);
  TIE(za);
  SCHED();
  ST(3);
  VMW(2);
  BAR();
  ST(4);
#pragma unroll
  for (int i = 0; i < 8; ++i) zf[i] = *(const u32x4*)(lds + za + i * 2048);
#pragma unroll
  for (int i = 0; i < 2; ++i) zx[i] = *(const u32x4*)(lds + (za ^ 64) + (2 * w + i) * 2048);
  SCHED();
  f32x4 tr[2] = {zero, zero}, ti[2] = {zero, zero};
#pragma unroll
  for (int a = 0; a < 4; ++a)
#pragma unroll
    for (int tb = 0; tb < 2; ++tb) { tr[tb] = mfma16(zf[2 * a + tb], Br[a], tr[tb]); ti[tb] = mfma16(zf[2 * a + tb], Bi[a], ti[tb]); }
  {
    f32x4 pr[2], pi[2];
#pragma unroll
    for (int j = 0; j < 2; ++j) {
      pr[j] = __builtin_elementwise_fma(ti[j], fs[j], tr[j] * fc[j]);
      pi[j] = __builtin_elementwise_fma(ti[j], fc[j], -(tr[j] * fs[j]));
    }
    const u32x4 Pr = {pk(pr[0][0], pr[0][1]), pk(pr[0][2], pr[0][3]), pk(pr[1][0], pr[1][1]), pk(pr[1][2], pr[1][3])};
    const u32x4 Pi = {pk(pi[0][0], pi[0][1]), pk(pi[0][2], pi[0][3]), pk(pi[1][0], pi[1][1]), pk(pi[1][2], pi[1][3])};
    const u32x4 Nr = Pr ^ NEG2;
    float yr[8], yi[8];
#pragma unroll
    for (int tt = 0; tt < 2; ++tt) {
      f32x4 xr = mfma16(C1[tt], Pr, zero), xi = mfma16(C1[tt], Pi, zero);
      xr = mfma16(S1[tt], Pi, xr); xi = mfma16(S1[tt], Nr, xi);
#pragma unroll
      for (int r = 0; r < 4; ++r) {
        const float a = lo16(wf[tt][r]), b = hi16(wf[tt][r]);
        yr[4 * tt + r] = xr[r] * a - xi[r] * b;
        yi[4 * tt + r] = xr[r] * b + xi[r] * a;
      }
    }
    const u32x4 Yr = {pk(yr[0], yr[1]), pk(yr[2], yr[3]), pk(yr[4], yr[5]), pk(yr[6], yr[7])};
    const u32x4 Yi = {pk(yi[0], yi[1]), pk(yi[2], yi[3]), pk(yi[4], yi[5]), pk(yi[6], yi[7])};
    const u32x4 Ni = Yi ^ NEG2;
#pragma unroll
    for (int tb = 0; tb < 2; ++tb) {
      f32x4 rr = mfma16(Yr, C1[tb], zero), ri = mfma16(Yr, S1[tb], zero);
      rr = mfma16(Ni, S1[tb], rr); ri = mfma16(Yi, C1[tb], ri);
      const f32x4 vr = __builtin_elementwise_fma(rr, ic[tb], -(ri * is[tb])), vi = __builtin_elementwise_fma(ri, ic[tb], rr * is[tb]);
      *(u32x4*)(lh + (w * 2 + tb) * 1024 + lane * 16) = u32x4{pk(vr[0], vr[1]), pk(vr[2], vr[3]), pk(vi[0], vi[1]), pk(vi[2], vi[3])};
    }
  }
  ST(1);
  __syncthreads();
  ST(2);
  u32x4 rf[8];
#pragma unroll
  for (int i = 0; i < 8; ++i) rf[i] = *(const u32x4*)(lh + i * 1024 + lane * 16);
  f32x4 ur[2], ui[2];
#pragma unroll
  for (int tb = 0; tb < 2; ++tb)
#pragma unroll
    for (int r = 0; r < 4; ++r) { ur[tb][r] = lo16(zx[tb][r]); ui[tb][r] = hi16(zx[tb][r]); }
  if (w == 0) k2_inv<0>(ur, ui, rf, Ar, Ai);
  else if (w == 1) k2_inv<1>(ur, ui, rf, Ar, Ai);
  else if (w == 2) k2_inv<2>(ur, ui, rf, Ar, Ai);
  else k2_inv<3>(ur, ui, rf, Ar, Ai);
  const uint32_t im = slot == 8 ? 0u : ~0u;
#pragma unroll
  for (int tb = 0; tb < 2; ++tb) {
    u32x4 o = {pk(ur[tb][0], ur[tb][1]), pk(ur[tb][2], ur[tb][3]), pk(ui[tb][0], ui[tb][1]) & im, pk(ui[tb][2], ui[tb][3]) & im};
    BST(o, rws, (l15 >> 1) * UQ + (l15 & 1) * 64 + g * 16, OFF_U + (16 * w + 8 * tb) * UQ + slot * 128, AUX_X);
  }
  ST_END(ws, 2);
}

#define ALOADC(dst, voff, rs, soff) asm volatile("buffer_load_dwordx4 %0, %1, %2, %3 offen sc1" : "=v"(dst) : "v"(voff), "s"(rs), "s"(soff) : "memory")
#define ALOAD1(dst, voff, rs, soff) asm volatile("buffer_load_dword %0, %1, %2, %3 offen" : "=v"(dst) : "v"(voff), "s"(rs), "s"(soff) : "memory")
#define ALOAD1C(dst, voff, rs, soff) asm volatile("buffer_load_dword %0, %1, %2, %3 offen sc1" : "=v"(dst) : "v"(voff), "s"(rs), "s"(soff) : "memory")
#define ADMA(ldsa, voff, rs, soff) asm volatile("s_mov_b32 m0, %0\n\ts_nop 0\n\tbuffer_load_dwordx4 %1, %2, %3 offen lds" ::"s"(ldsa), "v"(voff), "s"(rs), "s"(soff) : "memory")
#define VMWT(N, x) asm volatile("s_waitcnt vmcnt(%1)" : "+v"(x) : "n"(N) : "memory")
#define ARELOADC(dst, voff, rs, soff) asm volatile("buffer_load_dwordx4 %0, %1, %2, %3 offen sc1" : "+v"(dst) : "v"(voff), "s"(rs), "s"(soff) : "memory")
constexpr int RED = 272;
constexpr int L_SMALL = 32 * 16 * RED;
constexpr int L_CNT = L_SMALL + 2048 + 16384;
DI uint32_t umax(uint32_t a, uint32_t b) { return a > b ? a : b; }
DI uint32_t umin(uint32_t a, uint32_t b) { return a < b ? a : b; }
DI uint32_t max3u(uint32_t a, uint32_t b, uint32_t c) { return umax(umax(a, b), c); }

#define LAG1 12
#define LAG2 20
constexpr int step_q(int i) { const int u = i % 56, L = i < 56 ? LAG1 : LAG2; return 2 * (i / 56) + (u < L ? 0 : u >= 56 - L ? 1 : ((u - L) & 1) ^ 1); }
constexpr int step_k(int i) { const int u = i % 56, L = i < 56 ? LAG1 : LAG2; return u < L ? u : u >= 56 - L ? u - 28 : ((u - L) & 1) ? L + (u - L) / 2 : (u - L) / 2; }
#define NE 16
#define WE 13
#define R1M 3
#define R2M 3
#define SB3 87
#define FD 11
#define PD 8
#define CD 8
#define F2 1
#define F3 2
#define WD0 44
#define MK0 40
#define PR 99
#define ROT 0
#define GD2 1
constexpr int SB1 = 24, SB2 = 52, G0 = 57 - LAG1, G2 = 113 - LAG2, V1 = 53;
static_assert(SB3 <= 107 - LAG2 && SB3 > V1 + 32 && LAG1 >= 12 && LAG2 >= 20, "schedule");
constexpr int prio(int i) { return 3 - (i < SB1 ? i * 4 / SB1 : i < SB2 ? (i - SB1) * 4 / (SB2 - SB1) : i < SB3 ? (i - SB2) * 4 / (SB3 - SB2) : (i - SB3) * 4 / (112 - SB3)); }
constexpr int I_DM = 28, I_WD = 35, I_H0 = 63, I_H1 = 70, I_END = 77;
constexpr int I_PB = 90, I_FLAG = 94, I_POLL = 96, I_MK = 98, I_POLLR = 100;
constexpr int NS = 11, S_PB[2] = {G0 + 9, 75};
constexpr int st_of(int n) { return n >= 16 && n < 27 ? n - 16 : -1; }
constexpr int stage(int w, int j) { return j < 3 ? 4 * 28672 + (w * 3 + j) * 1024 : j < 5 ? L_SMALL + 2048 + (w * 2 + j - 3) * 1024 : (w * 6 + j - 5) * 1024; }
struct Sched { int at[112]; int cp[112]; int pos[101]; int cnt[113]; int next, ncp, last0; };
constexpr Sched make_sched() {
  Sched s{};
  int n = 0, nx = WE, nh = I_H0, used = 0, nc = 0;
  for (int k = 0; k < WE; ++k) s.pos[k] = n++;
  for (int i = 0; i < 112; ++i) {
    s.cnt[i] = n;
    int it = -1, room = NE - used;
    for (int j = 0; j <= i - 2; ++j) room += step_q(j) == 3;
    const int st = nx >= I_WD ? st_of(nx - I_WD) : -1;
    if (i == S_PB[0]) it = I_PB;
    else if (i == S_PB[1]) it = I_PB + 1;
    else if (i == G2 + 18 / GD2) it = I_PB + 2;
    else if (i == S_PB[0] + FD) it = I_FLAG;
    else if (i == S_PB[1] + FD) it = I_FLAG + 1;
    else if (i == S_PB[0] + FD + PD) it = I_POLL;
    else if (i == S_PB[1] + FD + PD) it = I_POLL + 1;
    else if (i == PR) it = I_POLLR;
    else if (i == MK0) it = I_MK;
    else if (i == MK0 + 2) it = I_MK + 1;
    else if (nx < I_WD) { if (i % R1M != R1M - 1) it = nx++; }
    else if (nh < I_END && nx >= I_WD + 16 && room > 0 && i >= S_PB[nh >= I_H1] + FD + PD + CD) { it = nh++; ++used; --room; }
    else if (nx < I_H0 && i >= WD0 && (i % R2M != R2M - 1 || i >= 76) && (st >= 0 ? (st < 5 || i > SB2) : room > 0)) {
      it = nx++;
      if (st < 0) { ++used; --room; }
    }
    s.at[i] = it;
    if (it >= 0) s.pos[it] = n++;
    s.cp[i] = -1;
    if (nx == I_H0 && nc < NS && room > 0 && (nh == I_END || 112 - i <= NS - nc) && i >= 96) { s.cp[i] = nc++; ++used; }
  }
  s.cnt[112] = n;
  s.next = nx == I_H0 ? nh : 0;
  s.ncp = nc;
  s.pos[I_END] = n;
  s.last0 = s.pos[I_H0 - 1] > s.pos[I_H1 - 1] ? s.pos[I_H0 - 1] : s.pos[I_H1 - 1];
  return s;
}
constexpr Sched SC = make_sched();
static_assert(SC.next == I_END && SC.ncp > 0 && SC.pos[I_POLLR] < SC.pos[I_END - 1] && SC.last0 < SC.pos[I_END - 1], "schedule");

__global__ __launch_bounds__(512, 2) void k3(char* __restrict__ ws, uint16_t* __restrict__ out) {
  __shared__ __attribute__((aligned(16))) char lds[L_CNT + 512];
  ST_BEGIN;
  const int t = threadIdx.x, lane = t & 63, l15 = lane & 15, g = lane >> 4;
  const int w = __builtin_amdgcn_readfirstlane(t >> 6);
  const int xcd = blockIdx.x & 7, jb = blockIdx.x >> 3;
  const int mt = 4 * (xcd >> 1) + (jb & 3), rp = xcd & 1, ct = jb >> 2, nt = 7 * rp + ct;
  const rsrc_t rws = mk(ws, WS_BYTES);
  const uint32_t vo = lane * 16;
  const int rf = (ROT & 1) && ct < 7 ? jb & 3 : 0, rd = (ROT & 2 ? jb & 3 : 0) ^ (ROT & 4 ? w & 3 : 0);
  const uint32_t lb = (uint32_t)(uintptr_t)(LDSP char*)lds + w * 7168, sU = OFF_U + mt * (4 * UQ) + (w ^ rf) * 7168;
#pragma unroll
  for (int n = 0; n < 7; ++n) ADMA(lb + n * 1024, vo, rws, sU + n * 1024);
  const int ml = lane & 31;
  uint32_t bo[2];
  bo[0] = (lane >> 5) * 512 + ((lane >> 3) & 1) * 128 + (g & 1) * 64 + (lane & 3) * 16 + ((lane >> 2) & 1) * 8;
  bo[1] = bo[0] + 2 * UQ;
  TIE(bo[0]); TIE(bo[1]);
  constexpr int PB = 4;
  if (ct == 7) {
    uint32_t mx[4];
#pragma unroll
    for (int q = 0; q < 4; ++q) ALOAD1(mx[q], ml * 4, rws, OFF_MX + (8 * mt + 2 * q) * 64);
    BAR();
    if (w == 7) {
      VMW(0);
      BAR();
    } else {
      const int j = 7 * rp + w;
      const uint32_t sE = OFF_E + j * (28 * 1024), sR = OFF_R + (mt * 14 + j) * 8192;
      constexpr int EE = 12;
      u32x4 e[28], bf[PB];
#pragma unroll
      for (int i = 0; i < EE; ++i) ALOAD(e[i], vo, rws, sE + i * 1024);
      asm volatile("s_waitcnt vmcnt(%4)" : "+v"(mx[0]), "+v"(mx[1]), "+v"(mx[2]), "+v"(mx[3]) : "n"(EE) : "memory");
      BAR();
      ST(1);
      f32x16 acc[4];
#pragma unroll
      for (int q = 0; q < 4; ++q)
#pragma unroll
        for (int r = 0; r < 16; ++r) acc[q][r] = __builtin_bit_cast(float, mx[q]);
      auto put = [&](int q) {
#pragma unroll
        for (int h = 0; h < 2; ++h)
          BST((u32x4{pk(acc[q][8 * h], acc[q][8 * h + 1]), pk(acc[q][8 * h + 2], acc[q][8 * h + 3]), pk(acc[q][8 * h + 4], acc[q][8 * h + 5]),
                     pk(acc[q][8 * h + 6], acc[q][8 * h + 7])}), rws, vo, sR + (q * 2 + h) * 1024, 16);
      };
#pragma unroll
      for (int i = 0; i < PB - 1; ++i) bf[i] = trpair(lds + bo[0] + (i & 1) * UQ + (i >> 1) * 1024, lds + bo[0] + (i & 1) * UQ + (i >> 1) * 1024 + 256);
      sfor<0, 112>([&](auto I) {
        constexpr int i = I, q = 2 * (i / 56) + (i & 1), ks = (i % 56) >> 1, NL = 28 - EE + 8;
        if constexpr (i == 52) { VMW(0); BAR(); }
        if constexpr (i + PB - 1 < 112) {
          constexpr int n = i + PB - 1, off = (n & 1) * UQ + ((n % 56) >> 1) * 1024;
          bf[n % PB] = trpair(lds + bo[n / 56] + off, lds + bo[n / 56] + off + 256);
        }
        if constexpr (q == 0) VMWT(EE + (i < NL ? i : NL) - ks - 1, e[ks]);
        acc[q] = mfma32(e[ks], bf[i % PB], acc[q]);
        if constexpr (i < 28 - EE) ALOAD(e[EE + i], vo, rws, sE + (EE + i) * 1024);
        else if constexpr (i < NL) ADMA(lb + 2 * UQ + (w + i - 28 + EE) * 1024, vo, rws, sU + 2 * UQ + (w + i - 28 + EE) * 1024);
        if constexpr (i == 60) put(0);
        if constexpr (i == 64) put(1);
        SCHED();
      });
      ST(2);
      put(2); put(3);
      VMW(0);
      if (lane == 0) __builtin_amdgcn_raw_buffer_store_b32(1u, rws, 0, OFF_FL + (224 + mt * 14 + j) * 128, 16);
    }
    ST_END(ws, 3);
    return;
  }
  u32x4 wf[28];
  const uint32_t sW = OFF_WGU + (nt * 8 + w) * (28 * 1024);
  const uint32_t sP = OFF_HH + (mt * 4 * 112 + nt * 8 + w) * 1024;
  uint32_t va = bo[0] + (w & 1) * UQ + (w >> 1) * 7168;
  TIE(va);
  if (w < 2) *(uint32_t*)(lds + L_CNT + w * 256 + lane * 4) = 0u;
  BAR();
#pragma unroll
  for (int i = 0; i < WE - 7; ++i) ALOAD(wf[i], vo, rws, sW + ((i / 7) ^ rf) * 7168 + i % 7 * 1024);
  ST(1);
  VMW(WE - 7);
  ST(2);
  asm volatile("s_waitcnt lgkmcnt(0)" ::: "memory");
  BAR();
  float sq[2];
  auto dot = [&](uint32_t x, int a, bool first) {
    const bf16x2 e0 = __builtin_bit_cast(bf16x2, x);
    sq[a] = __builtin_amdgcn_fdot2_f32_bf16(e0, e0, first ? 0.f : sq[a], false);
  };
  auto sq_put = [&](int pair) {
    const uint32_t x0 = __builtin_bit_cast(uint32_t, sq[0] + sq[1]);
    auto s = __builtin_amdgcn_permlane32_swap(x0, x0, false, false);
    *(float*)(lds + L_SMALL + pair * 1024 + (w & 1) * 512 + ml * 16 + (w >> 1) * 4) =
        __builtin_bit_cast(float, (uint32_t)s[0]) + __builtin_bit_cast(float, (uint32_t)s[1]);
  };
  {
    u32x4 vf[7];
#pragma unroll
    for (int i = 0; i < 7; ++i) vf[i] = trpair(lds + va + i * 1024, lds + va + i * 1024 + 256);
#pragma unroll
    for (int i = 0; i < 7; ++i) {
      ALOAD(wf[WE - 7 + i], vo, rws, sW + (((WE - 7 + i) / 7) ^ rf) * 7168 + (WE - 7 + i) % 7 * 1024);
#pragma unroll
      for (int j = 0; j < 4; ++j) dot(vf[i][j], j & 1, i == 0 && j < 2);
      SCHED();
    }
    sq_put(0);
  }
  __builtin_amdgcn_s_setprio(3);
  SCHED();
  ST(3);

  const uint32_t sD = OFF_WD + (ct * 56 + 7 * w) * 4096;
  const uint32_t sH = OFF_HH + (mt * 4 * 112 + 14 * w) * 1024 + ((ROT & 8 ? rp ^ w : rp) & 1) * 256, vH = g * 512 + l15 * 16;
  constexpr uint32_t HX = ROT & 8 ? 256 : 0;
  const uint32_t sF = OFF_FL + mt * (14 * 128);
  u32x4 wd[28], hh[4][7];
  uint32_t pf, pr;
  f32x16 acc[2];
  float o[8], rs[2], c1[2], gu;
  f32x4 ps;
  u32x4 bf[PB], vf[2];
  auto glu = [&](int p, int h) {
    const int e = h >> 1;
    if (h & 1) {
      if (e >= 1) { o[e - 1] *= gu; TIE(o[e - 1]); }
      if (e < 8) { o[e] = __builtin_amdgcn_rcpf(1.f + o[e]); TIE(o[e]); }
    } else {
      if (e >= 1) { gu = acc[p][7 + e] * rs[p]; TIE(gu); }
      if (e < 8) { o[e] = __builtin_amdgcn_exp2f(acc[p][e] * c1[p]); TIE(o[e]); }
    }
  };
  auto publish = [&](int q) {
    BST((u32x4{umin(pk(o[0], o[1]), SENT - 1), umin(pk(o[2], o[3]), SENT - 1), umin(pk(o[4], o[5]), SENT - 1), umin(pk(o[6], o[7]), SENT - 1)}),
        rws, vo ^ (w & 2 ? HX : 0), sP + q * (112 * 1024), AUX_H);
  };
  auto pollh = [&](int q) { ALOAD1C(pf, min((14 * w >> 3) + (lane & 3), (14 * w + 13) >> 3) * 128, rws, sF + q * 32); };
  auto pollr = [&]() { ALOAD1C(pr, (lane & 1) * 128, rws, sF + (224 + 2 * ct) * 128); };
  auto flagwait = [&](int q) {
    for (int spin = 0; __builtin_amdgcn_ballot_w64(pf == 0) != 0 && spin < (1 << 14); ++spin) {
      __builtin_amdgcn_s_sleep(1);
      pollh(q);
      VMWT(0, pf);
    }
  };
  uint32_t arr;
  uint32_t* const cnt = (uint32_t*)__builtin_assume_aligned(lds + L_CNT, 16) + lane;
  auto flag = [&](int q) {
    __builtin_amdgcn_raw_buffer_store_b32(1u, rws, lane == 0 && arr == 7 ? 0u : 0x80000000u, sF + nt * 128 + q * 32, 16);
  };
  const uint32_t l0 = (uint32_t)(uintptr_t)(LDSP char*)lds;
  auto issue = [&](auto IT) {
    constexpr int it = IT;
    if constexpr (it >= 0 && it < I_DM) ALOAD(wf[it], vo, rws, sW + ((it / 7) ^ rf) * 7168 + it % 7 * 1024);
    if constexpr (it >= I_DM && it < I_WD) ADMA(lb + 2 * UQ + (it - I_DM) * 1024, vo, rws, sU + 2 * UQ + (it - I_DM) * 1024);
    if constexpr (it >= I_WD && it < I_H0) {
      constexpr int k = it - I_WD;
      if constexpr (st_of(k) < 0) ALOAD(wd[k], vo, rws, sD + ((k / 7) ^ rd) * 1024 + k % 7 * 4096);
      else ADMA(l0 + stage(w, st_of(k)), vo, rws, sD + ((k / 7) ^ rd) * 1024 + k % 7 * 4096);
    }
    if constexpr (it >= I_H0 && it < I_END) ALOADC(hh[(it - I_H0) / 7][(it - I_H0) % 7], vH, rws, (sH ^ ((it - I_H0) % 7 & 1 ? HX : 0)) + (it - I_H0) / 7 * (112 * 1024) + (it - I_H0) % 7 * 2048);
    if constexpr (it >= I_PB && it < I_PB + 4) publish(it - I_PB);
    if constexpr (it == I_FLAG || it == I_FLAG + 1) flag(it - I_FLAG);
    if constexpr (it == I_POLL || it == I_POLL + 1) pollh(it - I_POLL);
    if constexpr (it == I_POLLR) pollr();
    if constexpr (it >= I_MK && it < I_MK + 2) BST((u32x4{SENT, SENT, SENT, SENT}), rws, vo, sP + (2 + it - I_MK) * (112 * 1024), AUX_H);
  };
  auto unstage = [&](auto J) { constexpr int j = J; wd[j + 16] = *(const u32x4*)(lds + stage(w, j) + vo); };
#pragma unroll
  for (int i = 0; i < PB - 1; ++i) bf[i] = trpair(lds + bo[0] + i * 1024, lds + bo[0] + i * 1024 + 256);
  sfor<0, 112>([&](auto I) {
    constexpr int i = I, u = i % 56, q = step_q(i), ks = step_k(i);
    if constexpr (i == SB2) { ST(4); VMW(SC.cnt[i] - SC.pos[I_WD - 1] - 1); BAR(); ST(5); }
    if constexpr (i == SB1) BAR();
    if constexpr (i == SB3) { STF(11); BAR(); ST(6); }
    if constexpr (i + PB - 1 < 112) {
      constexpr int n = i + PB - 1, off = (step_q(n) & 1) * UQ + step_k(n) * 1024;
      bf[n % PB] = trpair(lds + bo[n / 56] + off, lds + bo[n / 56] + off + 256);
    }
    constexpr int v = i - V1;
    if constexpr (v >= 0 && v < 28 && v % 4 == 0) vf[(v / 4) & 1] = trpair(lds + va + 2 * UQ + v * 256, lds + va + 2 * UQ + v * 256 + 256);
    if constexpr (q == 0) VMWT(SC.cnt[i] - SC.pos[ks] - 1, wf[ks]);
    if constexpr (i > 0 && prio(i) != prio(i - 1)) __builtin_amdgcn_s_setprio(prio(i));
    if constexpr (SC.at[i] == I_H0 || SC.at[i] == I_H1) {
      constexpr int fq = SC.at[i] == I_H1;
      if constexpr (!fq) { STF(12); }
      TIE(acc[0]); TIE(acc[1]); VMWT(SC.cnt[i] - SC.pos[I_POLL + fq] - 1, pf); flagwait(fq);
      if constexpr (!fq) { STF(13); }
      if constexpr (fq) { ST(7); }
    }
    if constexpr (ks == 0) {
      const f32x16 z = {0.f, 0.f, 0.f, 0.f, 0.f, 0.f, 0.f, 0.f, 0.f, 0.f, 0.f, 0.f, 0.f, 0.f, 0.f, 0.f};
      acc[q & 1] = mfma32(wf[ks], bf[i % PB], z);
    } else {
      acc[q & 1] = mfma32(wf[ks], bf[i % PB], acc[q & 1]);
    }
    if constexpr (v >= 3 && v < 31) { dot(vf[((v - 3) / 4) & 1][(v - 3) % 4], v & 1, v < 5); TIE(sq[v & 1]); }
    if constexpr (v == 31) sq_put(1);
    if constexpr (ks == 25) ps = *(const f32x4*)(lds + L_SMALL + q * 512 + ml * 16);
    if constexpr (ks == 27) { rs[q & 1] = __builtin_amdgcn_rsqf(((ps[0] + ps[1]) + (ps[2] + ps[3])) * (1.f / D) + 1e-5f); TIE(rs[q & 1]); }
    if constexpr (ks == 27) { c1[q & 1] = rs[q & 1] * -1.44269504088896f; }
    if constexpr (i >= G0 && i < G0 + 9) { glu(0, 2 * (i - G0)); glu(0, 2 * (i - G0) + 1); }
    if constexpr (i >= 57 && i < 75) glu(1, i - 57);
    if constexpr (i >= G2 && i < G2 + 18 / GD2) {
#pragma unroll
      for (int h = 0; h < GD2; ++h) glu(0, GD2 * (i - G2) + h);
    }
    if constexpr (i < 110 && (SC.at[i + 2] == I_FLAG || SC.at[i + 2] == I_FLAG + 1)) {
      constexpr int fq = SC.at[i + (i < 110 ? 2 : 0)] - I_FLAG;
      STF(1 + fq); VMW(SC.cnt[i] - SC.pos[I_PB + fq] - 1); STF(9 + fq);
      arr = __hip_atomic_fetch_add(cnt + fq * 64, 1u, __ATOMIC_RELAXED, __HIP_MEMORY_SCOPE_WORKGROUP);
    }
    issue(std::integral_constant<int, SC.at[i]>{});
    if constexpr (SC.cp[i] == 0) VMW(SC.cnt[i + 1] - SC.pos[I_H0 - 2] - 1);
    if constexpr (SC.cp[i] >= 0) unstage(std::integral_constant<int, SC.cp[i]>{});
    SCHED();
  });
  ST(8);
  sfor<SC.ncp, NS>(unstage);
  auto fetch = [&](int q, int i) { ALOADC(hh[q][i], vH, rws, (sH ^ (i & 1 ? HX : 0)) + q * (112 * 1024) + i * 2048); };
  auto check = [&](int q) {
#pragma unroll
    for (int i = 0; i < 7; ++i) TIE(hh[q][i]);
    for (int spin = 0; spin < (1 << 14); ++spin) {
      uint32_t m = 0;
#pragma unroll
      for (int i = 0; i < 7; ++i) m = max3u(max3u(m, hh[q][i][0], hh[q][i][1]), hh[q][i][2], hh[q][i][3]);
      if (__builtin_amdgcn_ballot_w64(m == SENT) == 0) break;
#pragma unroll
      for (int i = 0; i < 7; ++i) ARELOADC(hh[q][i], vH, rws, (sH ^ (i & 1 ? HX : 0)) + q * (112 * 1024) + i * 2048);
      VMW(0);
#pragma unroll
      for (int i = 0; i < 7; ++i) TIE(hh[q][i]);
    }
  };
  f32x4 a2[4][4];
  const f32x4 zero = {0.f, 0.f, 0.f, 0.f};
  auto gemm2 = [&](int q) {
#pragma unroll
    for (int i = 0; i < 7; ++i) {
#pragma unroll
      for (int cf = 0; cf < 4; ++cf) a2[q][cf] = mfma16(wd[7 * cf + i], hh[q][i], i ? a2[q][cf] : zero);
      if (q == 0) { glu(1, 2 * i + 4); glu(1, 2 * i + 5); }
      if (q == F2) fetch(2, i);
      if (q == F3) fetch(3, i);
      SCHED();
    }
  };
  auto spill = [&](int q) {
#pragma unroll
    for (int cf = 0; cf < 4; ++cf) *(f32x4*)(lds + ((q * 8 + w) * 16 + l15) * RED + (cf ^ rd) * 64 + g * 16) = a2[q][cf];
  };
  const int er = 2 * w + (lane >> 5), ec = 64 * ct + 2 * ml;
  uint32_t rc[4];
  const rsrc_t ro = mk(out, 2048 * 896);
  auto reduce = [&](int q) {
    f32x2 s = {lo16(rc[q]), hi16(rc[q])};
#pragma unroll
    for (int ww = 0; ww < 8; ++ww) s += *(const f32x2*)(lds + ((q * 8 + ww) * 16 + er) * RED + ml * 8);
    const int v = (int)pk(s[0], s[1]);
    const u32x4 ov = {(uint32_t)v, (uint32_t)__builtin_amdgcn_update_dpp(0, v, 0x55, 0xf, 0xf, true),
                      (uint32_t)__builtin_amdgcn_update_dpp(0, v, 0xaa, 0xf, 0xf, true),
                      (uint32_t)__builtin_amdgcn_update_dpp(0, v, 0xff, 0xf, 0xf, true)};
    BST(ov, ro, (lane & 3) == 0 ? er * (128 * 896) + ec * 2 : 0x80000000u, (8 * mt + 2 * q + rp) * 896, AUX_L);
  };
#pragma unroll
  for (int i = 0; i < 4; ++i) glu(1, i);
  VMW(SC.pos[I_END] - SC.last0 - 1);
#pragma unroll
  for (int i = 0; i < 28; ++i)
    if (st_of(i) < 0) TIE(wd[i]);
#pragma unroll
  for (int i = 0; i < 7; ++i) TIE(hh[0][i]);
  gemm2(0);
  publish(3);
  __builtin_amdgcn_s_setprio(0);
  STT(9);
  VMWT(SC.pos[I_END] - SC.pos[I_END - 1] + (F2 == 0 ? 7 : 0), pr);
#pragma unroll
  for (int i = 0; i < 7; ++i) TIE(hh[1][i]);
  for (int spin = 0; __builtin_amdgcn_ballot_w64(pr == 0) != 0 && spin < (1 << 14); ++spin) {
    __builtin_amdgcn_s_sleep(1);
    pollr();
    VMWT(0, pr);
  }
  {
    const uint32_t vR = (ml >> 4) * 8192 + ((ml >> 3) & 1) * 1024 + (((ml >> 1) & 1) * 32 + rp * 16 + er) * 16 + ((ml >> 2) & 1) * 8 + (ml & 1) * 4;
#pragma unroll
    for (int q = 0; q < 4; ++q) ALOAD1C(rc[q], vR, rws, OFF_R + (mt * 14 + 2 * ct) * 8192 + q * 2048);
  }
  STT(1);
  gemm2(1);
  STT(2);
  BAR();
  spill(0);
  spill(1);
  STT(10);
  SCHED();
  asm volatile("s_waitcnt vmcnt(%4)" : "+v"(rc[0]), "+v"(rc[1]), "+v"(rc[2]), "+v"(rc[3]) : "n"(F3 == 1 && F2 == 0 ? 7 : F3 == 1 ? 1 : 0) : "memory");
  check(2);
  STT(11);
  gemm2(2);
  spill(2);
  asm volatile("s_waitcnt lgkmcnt(0)" ::: "memory");
  BAR();
  STT(12);
  reduce(0); reduce(1); reduce(2);
  SCHED();
  VMW(3);
  check(3);
  STT(13);
  gemm2(3);
  spill(3);
  asm volatile("s_waitcnt lgkmcnt(0)" ::: "memory");
  BAR();
  reduce(3);
  ST_END(ws, 3);
}

extern "C" void kernel_launch(void* const* d_in, const int* in_sizes, int n_in, void* d_out, int out_size,
                              void* d_ws, size_t ws_size, hipStream_t stream) {
  const uint16_t* x = (const uint16_t*)d_in[0];
  const uint16_t* wr = (const uint16_t*)d_in[1];
  const uint16_t* wi = (const uint16_t*)d_in[2];
  const uint16_t* wg = (const uint16_t*)d_in[4];
  const uint16_t* wu = (const uint16_t*)d_in[5];
  const uint16_t* wd = (const uint16_t*)d_in[6];
  char* ws = (char*)d_ws;
  k1<<<256, 512, 0, stream>>>(x, wr, wi, wg, wu, ws);
  k2<<<252, 512, 0, stream>>>(wg, wu, wd, ws);
  k3<<<256, 512, 0, stream>>>(ws, (uint16_t*)d_out);
}
```

```cpp
#include <hip/hip_runtime.h>
#include <stdint.h>
#include <type_traits>

typedef __attribute__((ext_vector_type(8))) __bf16 bf16x8;
typedef __attribute__((ext_vector_type(2))) __bf16 bf16x2;
typedef __attribute__((ext_vector_type(2))) float f32x2;
typedef __attribute__((ext_vector_type(4))) float f32x4;
typedef __attribute__((ext_vector_type(16))) float f32x16;
typedef __attribute__((ext_vector_type(4))) uint32_t u32x4;
typedef __attribute__((ext_vector_type(2))) uint32_t u32x2;
typedef __attribute__((ext_vector_type(4))) short s16x4;
typedef __amdgpu_buffer_rsrc_t rsrc_t;
#define LDSP __attribute__((address_space(3)))
#define DI __device__ __forceinline__
#define SCHED() __builtin_amdgcn_sched_barrier(0)
#define TIE(x) asm volatile("" : "+v"(x))

#define AUX_X 16
#define AUX_L 16

#define ST(i)
#define STF(i)
#define STT(i)
#define STC(i)
#define ST_END(ws, kid)
#define ST_BEGIN

DI f32x4 mfma16(u32x4 a, u32x4 b, f32x4 c) {
  return __builtin_amdgcn_mfma_f32_16x16x32_bf16(__builtin_bit_cast(bf16x8, a), __builtin_bit_cast(bf16x8, b), c, 0, 0, 0);
}
DI f32x16 mfma32(u32x4 a, u32x4 b, f32x16 c) {
  return __builtin_amdgcn_mfma_f32_32x32x16_bf16(__builtin_bit_cast(bf16x8, a), __builtin_bit_cast(bf16x8, b), c, 0, 0, 0);
}
DI uint32_t pk(float a, float b) {
  f32x2 v = {a, b};
  return __builtin_bit_cast(uint32_t, __builtin_convertvector(v, bf16x2));
}
DI float lo16(uint32_t v) { return __builtin_bit_cast(float, v << 16); }
DI float hi16(uint32_t v) { return __builtin_bit_cast(float, v & 0xffff0000u); }
DI float cosr(float r) { return __builtin_amdgcn_cosf(r); }
DI float sinr(float r) { return __builtin_amdgcn_sinf(r); }
template <int N> DI float ror16(float v) {
  return __builtin_bit_cast(float, __builtin_amdgcn_update_dpp(0, __builtin_bit_cast(int, v), 0x120 + N, 0xf, 0xf, true));
}
DI float sum32(float v) {
  v += ror16<8>(v); v += ror16<4>(v); v += ror16<2>(v); v += ror16<1>(v);
  const uint32_t u = __builtin_bit_cast(uint32_t, v);
  auto s = __builtin_amdgcn_permlane16_swap(u, u, false, false);
  return __builtin_bit_cast(float, (uint32_t)s[0]) + __builtin_bit_cast(float, (uint32_t)s[1]);
}
DI rsrc_t mk(const void* p, uint32_t n) { return __builtin_amdgcn_make_buffer_rsrc((void*)p, 0, n, 0x00020000); }
DI u32x4 bld(rsrc_t r, uint32_t voff, uint32_t soff) { return __builtin_amdgcn_raw_buffer_load_b128(r, voff, soff, 0); }
#define BST(v, r, voff, soff, aux) do { const u32x4 bv_ = (v); __builtin_amdgcn_raw_buffer_store_b128(bv_, r, voff, soff, aux); asm volatile("s_nop 1" ::"v"(bv_) : "memory"); } while (0)
#define BLDSA(r, ldsptr, voff, soff, aux) __builtin_amdgcn_raw_ptr_buffer_load_lds(r, (LDSP void*)(ldsptr), 16, voff, soff, 0, aux)
#define BLDS(r, ldsptr, voff, soff) BLDSA(r, ldsptr, voff, soff, 0)
#define NT 13
#define AUX_H 16
DI u32x4 trpair(const char* p0, const char* p1) {
  s16x4 a0 = __builtin_amdgcn_ds_read_tr16_b64_v4i16((LDSP s16x4*)p0);
  s16x4 a1 = __builtin_amdgcn_ds_read_tr16_b64_v4i16((LDSP s16x4*)p1);
  u32x2 b0 = __builtin_bit_cast(u32x2, a0), b1 = __builtin_bit_cast(u32x2, a1);
  return u32x4{b0[0], b0[1], b1[0], b1[1]};
}

template <int I, int N, class Fn> DI void sfor(Fn&& f) {
  if constexpr (I < N) { f(std::integral_constant<int, I>{}); sfor<I + 1, N>(f); }
}

constexpr int D = 448, F = 1792;
constexpr uint32_t ZS = 28800;
constexpr uint32_t UQ = 28672;
constexpr uint32_t OFF_Z = 0;
constexpr uint32_t OFF_U = 26u << 20;
constexpr uint32_t OFF_MX = (8u << 20) - (256u << 10);
constexpr uint32_t OFF_FL = OFF_MX + 65536;
constexpr uint32_t OFF_WGU = 8u << 20;
constexpr uint32_t OFF_WD = 12u << 20;
constexpr uint32_t OFF_E = OFF_WD + 7 * 56 * 4096;
#define OVL 0
constexpr uint32_t OFF_HH = OVL ? 0 : 14u << 20;
constexpr uint32_t OFF_WF = OVL ? 128 * ZS : 22u << 20;
constexpr uint32_t OFF_R = 24u << 20;
constexpr uint32_t WS_BYTES = 28u << 20;
constexpr uint32_t NEG2 = 0x80008000u;
constexpr uint32_t SENT = 0xffffffffu;
constexpr float BETA = 0.06681531047810609f;
constexpr float RSQ2 = 0.7071067811865476f;
constexpr int GS = 2064;
#define ST_GRIDS {256, 252, 256}
#define ST_SPLITS {{128, 0, 0}, {112, 224, 0}, {224, 0, 0}}
#define CPF 1
#define VMW(N) asm volatile("s_waitcnt vmcnt(%0)" ::"n"(N) : "memory")
#define BAR() asm volatile("s_barrier" ::: "memory")
#define ALOAD(dst, voff, rs, soff) asm volatile("buffer_load_dwordx4 %0, %1, %2, %3 offen" : "=v"(dst) : "v"(voff), "s"(rs), "s"(soff) : "memory")
#define TOUCH(dst, voff, rs, soff) asm volatile("buffer_load_dword %0, %1, %2, %3 offen" : "=v"(dst) : "v"(voff), "s"(rs), "s"(soff) : "memory")
DI f32x2 cis(float r) { return f32x2{cosr(r), sinr(r)}; }
DI f32x2 cmul(f32x2 a, f32x2 b) { return __builtin_elementwise_fma(f32x2{a[1], a[0]}, f32x2{-b[1], b[1]}, a * b[0]); }

DI void dft28(int g, int l15, u32x4& Ec, u32x4& Es) {
  const float kf = (float)l15 * (1.f / 28.f);
  float cc[8], ss[8];
#pragma unroll
  for (int j = 0; j < 8; ++j) {
    const float a = (float)(4 * g + (j & 3) + 4 * (j & 4)) * kf;
    cc[j] = cosr(a); ss[j] = sinr(-a);
  }
#pragma unroll
  for (int q = 0; q < 4; ++q) { Ec[q] = pk(cc[2 * q], cc[2 * q + 1]); Es[q] = pk(ss[2 * q], ss[2 * q + 1]); }
  const uint32_t m3 = g == 3 ? 0u : ~0u;
  Ec[2] &= m3; Ec[3] &= m3; Es[2] &= m3; Es[3] &= m3;
}
template <int STEP> DI void dft16(int d0, int l15, float (&tc)[4], float (&ts)[4], u32x4& Tr, u32x4& Ti) {
  float c[4], s[4];
#pragma unroll
  for (int j = 0; j < 4; ++j) {
    const float a = (float)(d0 + STEP * j) * (float)l15;
    tc[j] = cosr(a * (1.f / 448.f)); ts[j] = sinr(a * (1.f / 448.f));
    c[j] = BETA * cosr(a * (1.f / 16.f)); s[j] = BETA * sinr(a * (1.f / 16.f));
  }
  Tr[0] = pk(c[0], c[1]); Tr[1] = pk(c[2], c[3]); Tr[2] = pk(s[0], s[1]); Tr[3] = pk(s[2], s[3]);
  Ti[0] = Tr[2] ^ NEG2; Ti[1] = Tr[3] ^ NEG2; Ti[2] = Tr[0]; Ti[3] = Tr[1];
}
DI int slot_group(int g, int l15) {
  return (l15 == 15 || (l15 == 14 && g >= 2) || (l15 == 0 && g < 2)) ? 14 : (l15 == 14 ? 0 : l15);
}

__global__ __launch_bounds__(512, 2) void k1(const uint16_t* __restrict__ x, const uint16_t* __restrict__ wr,
                                             const uint16_t* __restrict__ wi, const uint16_t* __restrict__ wg,
                                             const uint16_t* __restrict__ wu, char* __restrict__ ws) {
  constexpr int RS = 912;
  __shared__ __attribute__((aligned(16))) char lds[16 * RS + 15 * GS];
  ST_BEGIN;
  const int t = threadIdx.x, lane = t & 63, l15 = lane & 15, g = lane >> 4;
  const int w = __builtin_amdgcn_readfirstlane(t >> 6);
  const rsrc_t rws = mk(ws, WS_BYTES);
  if (blockIdx.x >= 128) {
    const int u = blockIdx.x - 128, kq = u & 3, gu = (u >> 2) & 3, tt = (u >> 4) & 1, rho = u >> 5;
    const rsrc_t rr = mk(wr, 2048 * 225 * 2), ri = mk(wi, 2048 * 225 * 2);
    {
      const int n = 2 * w + (lane >> 5);
      const int k = 4 * kq + (n & 3) + 16 * rho + 64 * (16 * tt + 4 * gu + (n >> 2));
      const uint32_t o0 = ((uint32_t)(k * 450) & ~15u) + (lane & 31) * 16;
      const uint32_t o1 = ((uint32_t)(((2048 - k) & 2047) * 450) & ~15u) + (lane & 31) * 16;
      BLDSA(rr, lds + w * 1024, o0, 0, NT & 2);
      BLDSA(rr, lds + 8192 + w * 1024, o1, 0, NT & 2);
      BLDSA(ri, lds + 16384 + w * 1024, o0, 0, NT & 2);
      BLDSA(ri, lds + 24576 + w * 1024, o1, 0, NT & 2);
    }
    const int id = (w & 3) * 128 + u;
    uint32_t pf = 0;
    {
      uint32_t lo, hi, n;
      asm volatile("s_getpc_b64 s[20:21]\n\ts_add_u32 %0, s20, _Z2k2PKtS0_S0_Pc@rel32@lo+4\n\ts_addc_u32 %1, s21, _Z2k2PKtS0_S0_Pc@rel32@hi+12\n\t"
                   "s_max_i32 %2, 0, k3_tail_a-_Z2k2PKtS0_S0_Pc\n\ts_max_i32 %2, %2, k3_tail_b-_Z2k2PKtS0_S0_Pc" : "=s"(lo), "=s"(hi), "=s"(n)::"s20", "s21", "scc");
      TOUCH(pf, lane * 128, mk((const void*)(((uint64_t)hi << 32) | lo), CPF && u < 16 ? n : 0u), w * 8192);
    }
    u32x4 eo;
    if (w >= 4) {
      const int ks = id % 28, d = 32 * (id / 28) + (lane & 31);
      const int kd1 = ks >= 2 ? ks >> 1 : (ks ? 0 : 14), e = 8 * (ks & 1) + 4 * (lane >> 5);
      const float a0 = (float)(kd1 * d) * (1.f / 448.f);
#pragma unroll
      for (int r = 0; r < 4; ++r) {
        const float a = a0 + (float)(((e + r) * d) & 15) * (1.f / 16.f);
        const float sc = (r == 0 && ks == 1 && lane < 32) ? BETA * RSQ2 : BETA;
        eo[r] = pk(sc * cosr(a), -sc * sinr(a));
      }
      TIE(eo);
    }
    const int i = lane & 3, e = lane >> 2;
    const int a0 = ((4 * kq + i) * 450) & 15, a1 = (16 - a0) & 15;
    ST(1);
    VMW(1);
    BAR();
    ST(2);
    if (w >= 4) {
      if (id < 392) BST(eo, rws, lane * 16, OFF_E + id * 1024, AUX_X);
      if (w == 4) BST((u32x4{0u, 0u, 0u, 0u}), rws, lane < 16 && u < 112 ? (lane >> 2) * 128 + (lane & 3) * 16 : 0x80000000u, OFF_FL + u * 512, AUX_X);
    }
#pragma unroll
    for (int n = 0; n < 2; ++n) {
      const int G = w + 8 * n;
      if (G < 14) {
        const int kd = (G ? G : (e < 8 ? 14 : 0)) + 28 * e;
        const bool mir = kd > 224;
        const char* p = lds + (mir ? 8192 + a1 + 2 * (448 - kd) : a0 + 2 * kd) + i * 512;
        const uint32_t sg = mir ? 0x80000000u : 0u;
        u32x4 o;
#pragma unroll
        for (int r = 0; r < 4; ++r) {
          const uint32_t re = *(const uint16_t*)(p + r * 2048), im = *(const uint16_t*)(p + 16384 + r * 2048);
          o[r] = (re | (im << 16)) ^ sg;
        }
        BST(o, rws, e * 8192 + i * 16, OFF_WF + G * (16 * 8192) + (rho * 2 + tt) * 1024 + (16 * gu + 4 * kq) * 16, AUX_X);
      }
    }
    ST(3);
    asm volatile("s_waitcnt vmcnt(0)" ::"v"(pf) : "memory");
    ST_END(ws, 1);
    return;
  }
  const int s2 = blockIdx.x;
  const int s1 = t >> 5, c = t & 31;
  const bool ok1 = c < 24;
  const rsrc_t rx = mk(x, 2048 * 896);
  u32x4 v0, v1;
  v0 = __builtin_amdgcn_raw_buffer_load_b128(rx, s1 * (128 * 896) + c * 16, s2 * 896, (NT & 1) * 2);
  v1 = __builtin_amdgcn_raw_buffer_load_b128(rx, s1 * (128 * 896) + (ok1 ? c + 32 : c) * 16, s2 * 896, (NT & 1) * 2);
  SCHED();
  u32x4 Ec, Es, Tr, Ti;
  float tc[4], ts[4];
  dft28(g, l15, Ec, Es);
  dft16<1>(4 * g, l15, tc, ts, Tr, Ti);
  uint32_t b0 = 2 * w * RS + lane * 8, b1 = b0 + ((g == 3) ? 0 : 512);
  uint32_t wa0 = s1 * RS + c * 16, wa1 = s1 * RS + (ok1 ? c * 16 + 512 : 896);
  uint32_t zw = 16 * RS + slot_group(g, l15) * GS + g * 512 + w * 16;
  uint32_t zr_ = 16 * RS + (w >> 1) * GS + (w & 1) * 1024 + (lane >> 3) * 128 + (lane & 3) * 32 + ((lane >> 2) & 1) * 8;
  uint32_t mo = c == 0 ? s1 * 4 : 0x80000000u;
  float k1m = ok1 ? 1.f : 0.f, m28 = l15 == 0 ? -28.f : 0.f, nyq = lane == 32 ? RSQ2 : 1.f;
#pragma unroll
  for (int i = 0; i < 4; ++i) { TIE(tc[i]); TIE(ts[i]); }
  TIE(Ec); TIE(Es); TIE(Tr); TIE(Ti); TIE(b0); TIE(b1); TIE(wa0); TIE(wa1); TIE(zw); TIE(zr_); TIE(mo); TIE(k1m); TIE(m28); TIE(nyq);
  SCHED();
  *(u32x4*)(lds + wa0) = v0;
  *(u32x4*)(lds + wa1) = v1;
  ST(1);
  const f32x4 zero = {0.f, 0.f, 0.f, 0.f};
  f32x4 xr[2], xi[2];
#pragma unroll
  for (int n = 0; n < 2; ++n) {
    const u32x4 a = trpair(lds + b0 + n * RS, lds + b1 + n * RS);
    xr[n] = mfma16(a, Ec, zero);
    xi[n] = mfma16(a, Es, zero);
  }
  float mean, rstd;
  {
    const bf16x2 one = {(__bf16)1.f, (__bf16)1.f};
    float a0 = 0.f, a1 = 0.f, q0 = 0.f, q1 = 0.f;
#pragma unroll
    for (int i = 0; i < 4; ++i) {
      const uint32_t u0 = v0[i], u1 = v1[i];
      const bf16x2 e0 = __builtin_bit_cast(bf16x2, u0), e1 = __builtin_bit_cast(bf16x2, u1);
      a0 = __builtin_amdgcn_fdot2_f32_bf16(e0, one, a0, false); a1 = __builtin_amdgcn_fdot2_f32_bf16(e1, one, a1, false);
      q0 = __builtin_amdgcn_fdot2_f32_bf16(e0, e0, q0, false); q1 = __builtin_amdgcn_fdot2_f32_bf16(e1, e1, q1, false);
    }
    mean = sum32(__builtin_fmaf(a1, k1m, a0)) * (1.f / D);
    const float var = __builtin_fmaxf(sum32(__builtin_fmaf(q1, k1m, q0)) * (1.f / D) - mean * mean, 0.f);
    rstd = __builtin_amdgcn_rsqf(var + 1e-5f);
    __builtin_amdgcn_raw_buffer_store_b32(__builtin_bit_cast(uint32_t, mean), rws, mo, OFF_MX + s2 * 64, AUX_X);
  }
  f32x4 zr[2], zi[2];
  float rs[2];
#pragma unroll
  for (int n = 0; n < 2; ++n) {
    const float mn = __builtin_bit_cast(float, __builtin_amdgcn_readlane(__builtin_bit_cast(int, mean), 32 * n)) * m28;
    rs[n] = __builtin_bit_cast(float, __builtin_amdgcn_readlane(__builtin_bit_cast(int, rstd), 32 * n));
    float tr[4], ti[4];
#pragma unroll
    for (int r = 0; r < 4; ++r) {
      tr[r] = __builtin_fmaf(xi[n][r], ts[r], __builtin_fmaf(xr[n][r], tc[r], mn));
      ti[r] = __builtin_fmaf(xi[n][r], tc[r], -(xr[n][r] * ts[r]));
    }
    const u32x4 b = {pk(tr[0], tr[1]), pk(tr[2], tr[3]), pk(ti[0], ti[1]), pk(ti[2], ti[3])};
    zr[n] = mfma16(Tr, b, zero);
    zi[n] = mfma16(Ti, b, zero);
  }
  zr[0][0] *= nyq; zr[1][0] *= nyq;
#pragma unroll
  for (int r = 0; r < 4; ++r)
    *(u32x4*)(lds + zw + r * 128) = u32x4{pk(zr[0][r] * rs[0], zi[0][r] * rs[0]), pk(zr[1][r] * rs[1], zi[1][r] * rs[1]),
                                          pk(zr[0][r], zi[0][r]), pk(zr[1][r], zi[1][r])};
  ST(2);
  asm volatile("s_waitcnt lgkmcnt(0)" ::: "memory");
  BAR();
  ST(3);
  u32x2 o0[4], o1[4];
#pragma unroll
  for (int i = 0; i < 4; ++i) {
    const int ii = i < 3 || w < 4 ? i : 2;
    o0[i] = *(const u32x2*)(lds + zr_ + ii * (4 * GS)); o1[i] = *(const u32x2*)(lds + zr_ + ii * (4 * GS) + 16);
  }
#pragma unroll
  for (int i = 0; i < 4; ++i)
    if (i < 3 || w < 4) BST((u32x4{o0[i][0], o0[i][1], o1[i][0], o1[i][1]}), rws, lane * 16, OFF_Z + s2 * ZS + (i * 8 + w) * 1024, AUX_X);
  ST_END(ws, 1);
}

template <int N> DI void rot_acc(f32x4& tr, f32x4& ti, u32x4 d, u32x4 P, u32x4 Q, u32x4 nP, u32x4 nQ) {
  const u32x4 cr = (N == 0) ? P : (N == 1) ? Q : (N == 2) ? nP : nQ;
  const u32x4 ci = (N == 0) ? Q : (N == 1) ? nP : (N == 2) ? nQ : P;
  tr = mfma16(cr, d, tr); ti = mfma16(ci, d, ti);
}
template <int KS> DI void k2_inv(f32x4 (&ur)[2], f32x4 (&ui)[2], const u32x4 (&rf)[8], u32x4 P, u32x4 Q) {
  const u32x4 nP = P ^ NEG2, nQ = Q ^ NEG2;
#pragma unroll
  for (int tb = 0; tb < 2; ++tb) rot_acc<0>(ur[tb], ui[tb], rf[tb], P, Q, nP, nQ);
#pragma unroll
  for (int tb = 0; tb < 2; ++tb) rot_acc<(4 - (KS & 3)) & 3>(ur[tb], ui[tb], rf[2 + tb], P, Q, nP, nQ);
#pragma unroll
  for (int tb = 0; tb < 2; ++tb) rot_acc<(4 - ((2 * KS) & 3)) & 3>(ur[tb], ui[tb], rf[4 + tb], P, Q, nP, nQ);
#pragma unroll
  for (int tb = 0; tb < 2; ++tb) rot_acc<(4 - ((3 * KS) & 3)) & 3>(ur[tb], ui[tb], rf[6 + tb], P, Q, nP, nQ);
}

__global__ __launch_bounds__(512, 2) void k2(const uint16_t* __restrict__ wg, const uint16_t* __restrict__ wu,
                                             const uint16_t* __restrict__ wd, char* __restrict__ ws) {
  __shared__ __attribute__((aligned(16))) char lds[28672 + 15 * GS];
  ST_BEGIN;
  const int t = threadIdx.x, lane = t & 63, l15 = lane & 15, g = lane >> 4;
  const int w8 = __builtin_amdgcn_readfirstlane(t >> 6), w = w8 & 3, hf = w8 >> 2;
  const rsrc_t rws = mk(ws, WS_BYTES);
  const f32x4 zero = {0.f, 0.f, 0.f, 0.f};
  const int q = (lane >> 2) & 3, p = lane & 3;
  if (blockIdx.x >= 224) {
    const int kb = blockIdx.x - 224;
    const rsrc_t rd = mk(wd, D * F * 2);
#pragma unroll
    for (int i = 0; i < 7; ++i) BLDSA(rd, lds + (i * 8 + w8) * 1024, lane * 16, kb * 57344 + (i * 8 + w8) * 1024, (NT >> 2) & 2);
    const char* b = lds + hf * 28672 + (16 * (g >> 1) + 4 * (g & 1) + q) * 896 + p * 8 + w * 32;
    u32x4 o[7];
    ST(1);
    __syncthreads();
    ST(2);
#pragma unroll
    for (int i = 0; i < 7; ++i) o[i] = trpair(b + i * 128, b + i * 128 + 8 * 896);
#pragma unroll
    for (int i = 0; i < 7; ++i) BST(o[i], rws, lane * 16, OFF_WD + ((i * 56 + 2 * kb + hf) * 4 + w) * 1024, AUX_X);
    ST_END(ws, 2);
    return;
  }
  if (blockIdx.x >= 112) {
    const int u = blockIdx.x - 112, pp = (u >> 4) * 8 + (u & 7), mat = pp & 1, fb = (pp & ~1) + ((u >> 3) & 1);
    const rsrc_t rw = mk(mat ? wu : wg, D * F * 2);
    const uint32_t vo = (lane >> 2) * (F * 2) + (((lane & 3) ^ w) << 4);
#pragma unroll
    for (int i = 0; i < 4; ++i)
      if (i < 3 || w8 < 4) BLDSA(rw, lds + (w8 + 8 * i) * 1024, vo, fb * 64 + (w8 + 8 * i) * (16 * F * 2), (NT >> 1) & 2);
    u32x4 Ec, Es, Tr, Ti;
    float tc[4], ts[4];
    dft28(g, l15, Ec, Es);
    dft16<4>(g, l15, tc, ts, Tr, Ti);
    uint32_t b0 = (4 * g + q) * 1024 + p * 64 + ((w ^ q) << 4) + hf * 8, b1 = b0 + (g == 3 ? 0 : 16384);
    uint32_t zw = slot_group(g, l15) * GS + g * 512 + w * 128 + hf * 64 + 28672;
    float nyq = lane == 32 ? RSQ2 : 1.f;
#pragma unroll
    for (int i = 0; i < 4; ++i) { TIE(tc[i]); TIE(ts[i]); }
    TIE(Ec); TIE(Es); TIE(Tr); TIE(Ti); TIE(b0); TIE(b1); TIE(zw); TIE(nyq);
    ST(1);
    __syncthreads();
    ST(2);
    u32x4 a[4];
#pragma unroll
    for (int tt = 0; tt < 4; ++tt) a[tt] = trpair(lds + b0 + tt * 256, lds + b1 + tt * 256);
    {
      f32x4 xr[4], xi[4];
#pragma unroll
      for (int tt = 0; tt < 4; ++tt) { xr[tt] = mfma16(a[tt], Ec, zero); xi[tt] = mfma16(a[tt], Es, zero); }
#pragma unroll
      for (int r = 0; r < 4; ++r) {
        float tr[4], ti[4];
#pragma unroll
        for (int tt = 0; tt < 4; ++tt) {
          tr[tt] = __builtin_fmaf(xi[tt][r], ts[tt], xr[tt][r] * tc[tt]);
          ti[tt] = __builtin_fmaf(xi[tt][r], tc[tt], -(xr[tt][r] * ts[tt]));
        }
        const u32x4 b = {pk(tr[0], tr[1]), pk(tr[2], tr[3]), pk(ti[0], ti[1]), pk(ti[2], ti[3])};
        const f32x4 zr = mfma16(Tr, b, zero), zi = mfma16(Ti, b, zero);
        *(u32x4*)(lds + zw + r * 16) = u32x4{pk(zr[0] * nyq, zi[0]), pk(zr[1], zi[1]), pk(zr[2], zi[2]), pk(zr[3], zi[3])};
      }
    }
    ST(3);
    __syncthreads();
    ST(4);
    const uint32_t so = ((lane >> 4) & 1) * 28672 + (lane >> 5) * 512 + l15 * 16;
#pragma unroll
    for (int i = 0; i < 4; ++i) a[i] = *(const u32x4*)(lds + 28672 + (w8 >> 1) * GS + (w8 & 1) * 1024 + lane * 16 + (i < 3 || w8 < 4 ? i : 2) * (4 * GS));
#pragma unroll
    for (int i = 0; i < 4; ++i)
      if (i < 3 || w8 < 4) BST(a[i], rws, so, OFF_WGU + fb * 57344 + mat * 256 + (w8 + 8 * i) * 1024, AUX_X);
    ST_END(ws, 2);
    return;
  }
  const int slot = (blockIdx.x & 7) * 28 + 2 * (blockIdx.x >> 3) + hf;
  char* const lh = lds + hf * 24576;
  u32x4 zf[8], wf[2], zx[2];
  {
    const uint32_t so = OFF_Z + slot * 128 + w * (32 * ZS), vz = (lane >> 3) * ZS + (((lane & 7) ^ (lane >> 4)) << 4);
#pragma unroll
    for (int i = 0; i < 4; ++i) BLDS(rws, lh + 8192 + (4 * w + i) * 1024, vz ^ ((i & 1) << 6), so + i * (8 * ZS));
    wf[0] = bld(rws, lane * 16, OFF_WF + (slot * 4 + w) * 2048);
    wf[1] = bld(rws, lane * 16, OFF_WF + (slot * 4 + w) * 2048 + 1024);
  }
  SCHED();
  u32x4 Br[4], Bi[4], Ar, Ai, C1[2], S1[2];
  f32x4 fc[2], fs[2], ic[2], is[2];
  {
    const float lf = (float)l15, g4 = (float)(4 * g), wf_ = (float)w;
    const float kc = lf + 16.f * wf_;
    {
      const f32x2 st = cis(kc * (1.f / 64.f));
      f32x2 z = cis(g4 * (kc * (1.f / 16.f)));
#pragma unroll
      for (int n = 0; n < 16; ++n) {
        Br[n & 3][n >> 2] = pk(z[0], z[1]); Bi[n & 3][n >> 2] = pk(-z[1], z[0]);
        z = cmul(z, st);
      }
    }
    {
      const f32x2 st = cis(kc * (1.f / 2048.f)), jp = cis(kc * (1.f / 128.f));
      f32x2 z = cis(g4 * (kc * (1.f / 2048.f)));
#pragma unroll
      for (int j = 0; j < 4; ++j) {
        const f32x2 y = cmul(z, jp);
        fc[0][j] = z[0]; fs[0][j] = z[1]; fc[1][j] = y[0]; fs[1][j] = y[1];
        z = cmul(z, st);
      }
    }
    const uint32_t lsg = (l15 & 1) ? NEG2 : 0u;
    {
      const f32x2 st = cis(lf * (1.f / 32.f)), z0 = cis(g4 * (lf * (1.f / 32.f))), z1 = cmul(z0, st), z2 = cmul(z1, st), z3 = cmul(z2, st);
      C1[0][0] = pk(z0[0], z1[0]); C1[0][1] = pk(z2[0], z3[0]); S1[0][0] = pk(z0[1], z1[1]); S1[0][1] = pk(z2[1], z3[1]);
#pragma unroll
      for (int q2 = 0; q2 < 2; ++q2) { C1[0][q2 + 2] = C1[0][q2] ^ lsg; S1[0][q2 + 2] = S1[0][q2] ^ lsg; }
    }
    C1[1] = C1[0] ^ 0x80000000u; S1[1] = S1[0] ^ 0x80000000u;
#pragma unroll
    for (int h = 0; h < 2; ++h) {
      const float b = (lf + (float)(16 * h)) * (1.f / 2048.f);
      const f32x2 st = cis(b);
      f32x2 z = cis((g4 + 16.f * wf_) * b) * (1.f / 2048.f);
#pragma unroll
      for (int j = 0; j < 4; ++j) { ic[h][j] = z[0]; is[h][j] = z[1]; z = cmul(z, st); }
    }
    {
      const float b = (4.f * lf + wf_) * (1.f / 64.f);
      const f32x2 st = cis(b), z0 = cis(g4 * b), z1 = cmul(z0, st), z2 = cmul(z1, st), z3 = cmul(z2, st);
      Ar[0] = pk(z0[0], z1[0]); Ar[1] = pk(z2[0], z3[0]);
      Ai[0] = pk(z0[1], z1[1]); Ai[1] = pk(z2[1], z3[1]);
    }
    Ar[2] = Ai[0] ^ NEG2; Ar[3] = Ai[1] ^ NEG2; Ai[2] = Ar[0]; Ai[3] = Ar[1];
#pragma unroll
    for (int j = 0; j < 2; ++j) { TIE(fc[j]); TIE(fs[j]); TIE(ic[j]); TIE(is[j]); }
    TIE(Ar); TIE(Ai);
  }
  uint32_t za = hf * 24576 + 8192 + l15 * 128 + ((g ^ (l15 >> 1)) << 4);
  TIE(za);
  SCHED();
  ST(3);
  VMW(2);
  BAR();
  ST(4);
#pragma unroll
  for (int i = 0; i < 8; ++i) zf[i] = *(const u32x4*)(lds + za + i * 2048);
#pragma unroll
  for (int i = 0; i < 2; ++i) zx[i] = *(const u32x4*)(lds + (za ^ 64) + (2 * w + i) * 2048);
  SCHED();
  f32x4 tr[2] = {zero, zero}, ti[2] = {zero, zero};
#pragma unroll
  for (int a = 0; a < 4; ++a)
#pragma unroll
    for (int tb = 0; tb < 2; ++tb) { tr[tb] = mfma16(zf[2 * a + tb], Br[a], tr[tb]); ti[tb] = mfma16(zf[2 * a + tb], Bi[a], ti[tb]); }
  {
    f32x4 pr[2], pi[2];
#pragma unroll
    for (int j = 0; j < 2; ++j) {
      pr[j] = __builtin_elementwise_fma(ti[j], fs[j], tr[j] * fc[j]);
      pi[j] = __builtin_elementwise_fma(ti[j], fc[j], -(tr[j] * fs[j]));
    }
    const u32x4 Pr = {pk(pr[0][0], pr[0][1]), pk(pr[0][2], pr[0][3]), pk(pr[1][0], pr[1][1]), pk(pr[1][2], pr[1][3])};
    const u32x4 Pi = {pk(pi[0][0], pi[0][1]), pk(pi[0][2], pi[0][3]), pk(pi[1][0], pi[1][1]), pk(pi[1][2], pi[1][3])};
    const u32x4 Nr = Pr ^ NEG2;
    float yr[8], yi[8];
#pragma unroll
    for (int tt = 0; tt < 2; ++tt) {
      f32x4 xr = mfma16(C1[tt], Pr, zero), xi = mfma16(C1[tt], Pi, zero);
      xr = mfma16(S1[tt], Pi, xr); xi = mfma16(S1[tt], Nr, xi);
#pragma unroll
      for (int r = 0; r < 4; ++r) {
        const float a = lo16(wf[tt][r]), b = hi16(wf[tt][r]);
        yr[4 * tt + r] = xr[r] * a - xi[r] * b;
        yi[4 * tt + r] = xr[r] * b + xi[r] * a;
      }
    }
    const u32x4 Yr = {pk(yr[0], yr[1]), pk(yr[2], yr[3]), pk(yr[4], yr[5]), pk(yr[6], yr[7])};
    const u32x4 Yi = {pk(yi[0], yi[1]), pk(yi[2], yi[3]), pk(yi[4], yi[5]), pk(yi[6], yi[7])};
    const u32x4 Ni = Yi ^ NEG2;
#pragma unroll
    for (int tb = 0; tb < 2; ++tb) {
      f32x4 rr = mfma16(Yr, C1[tb], zero), ri = mfma16(Yr, S1[tb], zero);
      rr = mfma16(Ni, S1[tb], rr); ri = mfma16(Yi, C1[tb], ri);
      const f32x4 vr = __builtin_elementwise_fma(rr, ic[tb], -(ri * is[tb])), vi = __builtin_elementwise_fma(ri, ic[tb], rr * is[tb]);
      *(u32x4*)(lh + (w * 2 + tb) * 1024 + lane * 16) = u32x4{pk(vr[0], vr[1]), pk(vr[2], vr[3]), pk(vi[0], vi[1]), pk(vi[2], vi[3])};
    }
  }
  ST(1);
  __syncthreads();
  ST(2);
  u32x4 rf[8];
#pragma unroll
  for (int i = 0; i < 8; ++i) rf[i] = *(const u32x4*)(lh + i * 1024 + lane * 16);
  f32x4 ur[2], ui[2];
#pragma unroll
  for (int tb = 0; tb < 2; ++tb)
#pragma unroll
    for (int r = 0; r < 4; ++r) { ur[tb][r] = lo16(zx[tb][r]); ui[tb][r] = hi16(zx[tb][r]); }
  if (w == 0) k2_inv<0>(ur, ui, rf, Ar, Ai);
  else if (w == 1) k2_inv<1>(ur, ui, rf, Ar, Ai);
  else if (w == 2) k2_inv<2>(ur, ui, rf, Ar, Ai);
  else k2_inv<3>(ur, ui, rf, Ar, Ai);
  const uint32_t im = slot == 8 ? 0u : ~0u;
#pragma unroll
  for (int tb = 0; tb < 2; ++tb) {
    u32x4 o = {pk(ur[tb][0], ur[tb][1]), pk(ur[tb][2], ur[tb][3]), pk(ui[tb][0], ui[tb][1]) & im, pk(ui[tb][2], ui[tb][3]) & im};
    BST(o, rws, (l15 >> 1) * UQ + (l15 & 1) * 64 + g * 16, OFF_U + (16 * w + 8 * tb) * UQ + slot * 128, AUX_X);
  }
  ST_END(ws, 2);
}

#define ALOADC(dst, voff, rs, soff) asm volatile("buffer_load_dwordx4 %0, %1, %2, %3 offen sc1" : "=v"(dst) : "v"(voff), "s"(rs), "s"(soff) : "memory")
#define ALOAD1(dst, voff, rs, soff) asm volatile("buffer_load_dword %0, %1, %2, %3 offen" : "=v"(dst) : "v"(voff), "s"(rs), "s"(soff) : "memory")
#define ALOAD1C(dst, voff, rs, soff) asm volatile("buffer_load_dword %0, %1, %2, %3 offen sc1" : "=v"(dst) : "v"(voff), "s"(rs), "s"(soff) : "memory")
#define ADMA(ldsa, voff, rs, soff) asm volatile("s_mov_b32 m0, %0\n\ts_nop 0\n\tbuffer_load_dwordx4 %1, %2, %3 offen lds" ::"s"(ldsa), "v"(voff), "s"(rs), "s"(soff) : "memory")
#define VMWT(N, x) asm volatile("s_waitcnt vmcnt(%1)" : "+v"(x) : "n"(N) : "memory")
#define ARELOADC(dst, voff, rs, soff) asm volatile("buffer_load_dwordx4 %0, %1, %2, %3 offen sc1" : "+v"(dst) : "v"(voff), "s"(rs), "s"(soff) : "memory")
constexpr int RED = 272;
constexpr int L_SMALL = 32 * 16 * RED;
constexpr int L_CNT = L_SMALL + 2048 + 16384;
DI uint32_t umax(uint32_t a, uint32_t b) { return a > b ? a : b; }
DI uint32_t umin(uint32_t a, uint32_t b) { return a < b ? a : b; }
DI uint32_t max3u(uint32_t a, uint32_t b, uint32_t c) { return umax(umax(a, b), c); }

#define LAG1 12
#define LAG2 20
constexpr int step_q(int i) { const int u = i % 56, L = i < 56 ? LAG1 : LAG2; return 2 * (i / 56) + (u < L ? 0 : u >= 56 - L ? 1 : ((u - L) & 1) ^ 1); }
constexpr int step_k(int i) { const int u = i % 56, L = i < 56 ? LAG1 : LAG2; return u < L ? u : u >= 56 - L ? u - 28 : ((u - L) & 1) ? L + (u - L) / 2 : (u - L) / 2; }
#define NE 16
#define WE 13
#define R1M 3
#define R2M 3
#define SB3 87
#define FD 11
#define PD 8
#define CD 8
#define F2 0
#define F3 2
#define WD0 44
#define MK0 40
#define PR 99
#define ROT 0
#define GD2 2
constexpr int SB1 = 24, SB2 = 52, G0 = 57 - LAG1, G2 = 113 - LAG2, V1 = 53;
static_assert(SB3 <= 107 - LAG2 && SB3 > V1 + 32 && LAG1 >= 12 && LAG2 >= 20, "schedule");
constexpr int prio(int i) { return 3 - (i < SB1 ? i * 4 / SB1 : i < SB2 ? (i - SB1) * 4 / (SB2 - SB1) : i < SB3 ? (i - SB2) * 4 / (SB3 - SB2) : (i - SB3) * 4 / (112 - SB3)); }
constexpr int I_DM = 28, I_WD = 35, I_H0 = 63, I_H1 = 70, I_END = 77;
constexpr int I_PB = 90, I_FLAG = 94, I_POLL = 96, I_MK = 98, I_POLLR = 100;
constexpr int NS = 11, S_PB[2] = {G0 + 9, 75};
constexpr int st_of(int n) { return n >= 16 && n < 27 ? n - 16 : -1; }
constexpr int stage(int w, int j) { return j < 3 ? 4 * 28672 + (w * 3 + j) * 1024 : j < 5 ? L_SMALL + 2048 + (w * 2 + j - 3) * 1024 : (w * 6 + j - 5) * 1024; }
struct Sched { int at[112]; int cp[112]; int pos[101]; int cnt[113]; int next, ncp, last0; };
constexpr Sched make_sched() {
  Sched s{};
  int n = 0, nx = WE, nh = I_H0, used = 0, nc = 0;
  for (int k = 0; k < WE; ++k) s.pos[k] = n++;
  for (int i = 0; i < 112; ++i) {
    s.cnt[i] = n;
    int it = -1, room = NE - used;
    for (int j = 0; j <= i - 2; ++j) room += step_q(j) == 3;
    const int st = nx >= I_WD ? st_of(nx - I_WD) : -1;
    if (i == S_PB[0]) it = I_PB;
    else if (i == S_PB[1]) it = I_PB + 1;
    else if (i == G2 + 18 / GD2) it = I_PB + 2;
    else if (i == S_PB[0] + FD) it = I_FLAG;
    else if (i == S_PB[1] + FD) it = I_FLAG + 1;
    else if (i == S_PB[0] + FD + PD) it = I_POLL;
    else if (i == S_PB[1] + FD + PD) it = I_POLL + 1;
    else if (i == PR) it = I_POLLR;
    else if (i == MK0) it = I_MK;
    else if (i == MK0 + 2) it = I_MK + 1;
    else if (nx < I_WD) { if (i % R1M != R1M - 1) it = nx++; }
    else if (nh < I_END && nx >= I_WD + 16 && room > 0 && i >= S_PB[nh >= I_H1] + FD + PD + CD) { it = nh++; ++used; --room; }
    else if (nx < I_H0 && i >= WD0 && (i % R2M != R2M - 1 || i >= 76) && (st >= 0 ? (st < 5 || i > SB2) : room > 0)) {
      it = nx++;
      if (st < 0) { ++used; --room; }
    }
    s.at[i] = it;
    if (it >= 0) s.pos[it] = n++;
    s.cp[i] = -1;
    if (nx == I_H0 && nc < NS && room > 0 && (nh == I_END || 112 - i <= NS - nc) && i >= 96) { s.cp[i] = nc++; ++used; }
  }
  s.cnt[112] = n;
  s.next = nx == I_H0 ? nh : 0;
  s.ncp = nc;
  s.pos[I_END] = n;
  s.last0 = s.pos[I_H0 - 1] > s.pos[I_H1 - 1] ? s.pos[I_H0 - 1] : s.pos[I_H1 - 1];
  return s;
}
constexpr Sched SC = make_sched();
static_assert(SC.next == I_END && SC.ncp > 0 && SC.pos[I_POLLR] < SC.pos[I_END - 1] && SC.last0 < SC.pos[I_END - 1], "schedule");

__global__ __launch_bounds__(512, 2) void k3(char* __restrict__ ws, uint16_t* __restrict__ out) {
  __shared__ __attribute__((aligned(16))) char lds[L_CNT + 512];
  ST_BEGIN;
  const int t = threadIdx.x, lane = t & 63, l15 = lane & 15, g = lane >> 4;
  const int w = __builtin_amdgcn_readfirstlane(t >> 6);
  const int xcd = blockIdx.x & 7, jb = blockIdx.x >> 3;
  const int mt = 4 * (xcd >> 1) + (jb & 3), rp = xcd & 1, ct = jb >> 2, nt = 7 * rp + ct;
  const rsrc_t rws = mk(ws, WS_BYTES);
  const uint32_t vo = lane * 16;
  const int rf = (ROT & 1) && ct < 7 ? jb & 3 : 0, rd = (ROT & 2 ? jb & 3 : 0) ^ (ROT & 4 ? w & 3 : 0);
  const uint32_t lb = (uint32_t)(uintptr_t)(LDSP char*)lds + w * 7168, sU = OFF_U + mt * (4 * UQ) + (w ^ rf) * 7168;
#pragma unroll
  for (int n = 0; n < 7; ++n) ADMA(lb + n * 1024, vo, rws, sU + n * 1024);
  const int ml = lane & 31;
  uint32_t bo[2];
  bo[0] = (lane >> 5) * 512 + ((lane >> 3) & 1) * 128 + (g & 1) * 64 + (lane & 3) * 16 + ((lane >> 2) & 1) * 8;
  bo[1] = bo[0] + 2 * UQ;
  TIE(bo[0]); TIE(bo[1]);
  constexpr int PB = 4;
  if (ct == 7) {
    uint32_t mx[4];
#pragma unroll
    for (int q = 0; q < 4; ++q) ALOAD1(mx[q], ml * 4, rws, OFF_MX + (8 * mt + 2 * q) * 64);
    BAR();
    if (w == 7) {
      VMW(0);
      BAR();
    } else {
      const int j = 7 * rp + w;
      const uint32_t sE = OFF_E + j * (28 * 1024), sR = OFF_R + (mt * 14 + j) * 8192;
      constexpr int EE = 12;
      u32x4 e[28], bf[PB];
#pragma unroll
      for (int i = 0; i < EE; ++i) ALOAD(e[i], vo, rws, sE + i * 1024);
      asm volatile("s_waitcnt vmcnt(%4)" : "+v"(mx[0]), "+v"(mx[1]), "+v"(mx[2]), "+v"(mx[3]) : "n"(EE) : "memory");
      BAR();
      ST(1);
      f32x16 acc[4];
#pragma unroll
      for (int q = 0; q < 4; ++q)
#pragma unroll
        for (int r = 0; r < 16; ++r) acc[q][r] = __builtin_bit_cast(float, mx[q]);
      auto put = [&](int q) {
#pragma unroll
        for (int h = 0; h < 2; ++h)
          BST((u32x4{pk(acc[q][8 * h], acc[q][8 * h + 1]), pk(acc[q][8 * h + 2], acc[q][8 * h + 3]), pk(acc[q][8 * h + 4], acc[q][8 * h + 5]),
                     pk(acc[q][8 * h + 6], acc[q][8 * h + 7])}), rws, vo, sR + (q * 2 + h) * 1024, 16);
      };
#pragma unroll
      for (int i = 0; i < PB - 1; ++i) bf[i] = trpair(lds + bo[0] + (i & 1) * UQ + (i >> 1) * 1024, lds + bo[0] + (i & 1) * UQ + (i >> 1) * 1024 + 256);
      sfor<0, 112>([&](auto I) {
        constexpr int i = I, q = 2 * (i / 56) + (i & 1), ks = (i % 56) >> 1, NL = 28 - EE + 8;
        if constexpr (i == 52) { VMW(0); BAR(); }
        if constexpr (i + PB - 1 < 112) {
          constexpr int n = i + PB - 1, off = (n & 1) * UQ + ((n % 56) >> 1) * 1024;
          bf[n % PB] = trpair(lds + bo[n / 56] + off, lds + bo[n / 56] + off + 256);
        }
        if constexpr (q == 0) VMWT(EE + (i < NL ? i : NL) - ks - 1, e[ks]);
        acc[q] = mfma32(e[ks], bf[i % PB], acc[q]);
        if constexpr (i < 28 - EE) ALOAD(e[EE + i], vo, rws, sE + (EE + i) * 1024);
        else if constexpr (i < NL) ADMA(lb + 2 * UQ + (w + i - 28 + EE) * 1024, vo, rws, sU + 2 * UQ + (w + i - 28 + EE) * 1024);
        if constexpr (i == 60) put(0);
        if constexpr (i == 64) put(1);
        SCHED();
      });
      ST(2);
      put(2); put(3);
      asm volatile("k3_tail_b:");
      VMW(0);
      if (lane == 0) __builtin_amdgcn_raw_buffer_store_b32(1u, rws, 0, OFF_FL + (224 + mt * 14 + j) * 128, 16);
    }
    ST_END(ws, 3);
    return;
  }
  u32x4 wf[28];
  const uint32_t sW = OFF_WGU + (nt * 8 + w) * (28 * 1024);
  const uint32_t sP = OFF_HH + (mt * 4 * 112 + nt * 8 + w) * 1024;
  uint32_t va = bo[0] + (w & 1) * UQ + (w >> 1) * 7168;
  TIE(va);
  if (w < 2) *(uint32_t*)(lds + L_CNT + w * 256 + lane * 4) = 0u;
  BAR();
#pragma unroll
  for (int i = 0; i < WE - 7; ++i) ALOAD(wf[i], vo, rws, sW + ((i / 7) ^ rf) * 7168 + i % 7 * 1024);
  ST(1);
  VMW(WE - 7);
  ST(2);
  asm volatile("s_waitcnt lgkmcnt(0)" ::: "memory");
  BAR();
  float sq[2];
  auto dot = [&](uint32_t x, int a, bool first) {
    const bf16x2 e0 = __builtin_bit_cast(bf16x2, x);
    sq[a] = __builtin_amdgcn_fdot2_f32_bf16(e0, e0, first ? 0.f : sq[a], false);
  };
  auto sq_put = [&](int pair) {
    const uint32_t x0 = __builtin_bit_cast(uint32_t, sq[0] + sq[1]);
    auto s = __builtin_amdgcn_permlane32_swap(x0, x0, false, false);
    *(float*)(lds + L_SMALL + pair * 1024 + (w & 1) * 512 + ml * 16 + (w >> 1) * 4) =
        __builtin_bit_cast(float, (uint32_t)s[0]) + __builtin_bit_cast(float, (uint32_t)s[1]);
  };
  {
    u32x4 vf[7];
#pragma unroll
    for (int i = 0; i < 7; ++i) vf[i] = trpair(lds + va + i * 1024, lds + va + i * 1024 + 256);
#pragma unroll
    for (int i = 0; i < 7; ++i) {
      ALOAD(wf[WE - 7 + i], vo, rws, sW + (((WE - 7 + i) / 7) ^ rf) * 7168 + (WE - 7 + i) % 7 * 1024);
#pragma unroll
      for (int j = 0; j < 4; ++j) dot(vf[i][j], j & 1, i == 0 && j < 2);
      SCHED();
    }
    sq_put(0);
  }
  __builtin_amdgcn_s_setprio(3);
  SCHED();
  ST(3);

  const uint32_t sD = OFF_WD + (ct * 56 + 7 * w) * 4096;
  const uint32_t sH = OFF_HH + (mt * 4 * 112 + 14 * w) * 1024 + ((ROT & 8 ? rp ^ w : rp) & 1) * 256, vH = g * 512 + l15 * 16;
  constexpr uint32_t HX = ROT & 8 ? 256 : 0;
  const uint32_t sF = OFF_FL + mt * (14 * 128);
  u32x4 wd[28], hh[4][7];
  uint32_t pf, pr;
  f32x16 acc[2];
  float o[8], rs[2], c1[2], gu;
  f32x4 ps;
  u32x4 bf[PB], vf[2];
  auto glu = [&](int p, int h) {
    const int e = h >> 1;
    if (h & 1) {
      if (e >= 1) { o[e - 1] *= gu; TIE(o[e - 1]); }
      if (e < 8) { o[e] = __builtin_amdgcn_rcpf(1.f + o[e]); TIE(o[e]); }
    } else {
      if (e >= 1) { gu = acc[p][7 + e] * rs[p]; TIE(gu); }
      if (e < 8) { o[e] = __builtin_amdgcn_exp2f(acc[p][e] * c1[p]); TIE(o[e]); }
    }
  };
  auto publish = [&](int q) {
    BST((u32x4{umin(pk(o[0], o[1]), SENT - 1), umin(pk(o[2], o[3]), SENT - 1), umin(pk(o[4], o[5]), SENT - 1), umin(pk(o[6], o[7]), SENT - 1)}),
        rws, vo ^ (w & 2 ? HX : 0), sP + q * (112 * 1024), AUX_H);
  };
  auto pollh = [&](int q) { ALOAD1C(pf, min((14 * w >> 3) + (lane & 3), (14 * w + 13) >> 3) * 128, rws, sF + q * 32); };
  auto pollr = [&]() { ALOAD1C(pr, (lane & 1) * 128, rws, sF + (224 + 2 * ct) * 128); };
  auto flagwait = [&](int q) {
    for (int spin = 0; __builtin_amdgcn_ballot_w64(pf == 0) != 0 && spin < (1 << 14); ++spin) {
      __builtin_amdgcn_s_sleep(1);
      pollh(q);
      VMWT(0, pf);
    }
  };
  uint32_t arr;
  uint32_t* const cnt = (uint32_t*)__builtin_assume_aligned(lds + L_CNT, 16) + lane;
  auto flag = [&](int q) {
    __builtin_amdgcn_raw_buffer_store_b32(1u, rws, lane == 0 && arr == 7 ? 0u : 0x80000000u, sF + nt * 128 + q * 32, 16);
  };
  const uint32_t l0 = (uint32_t)(uintptr_t)(LDSP char*)lds;
  auto issue = [&](auto IT) {
    constexpr int it = IT;
    if constexpr (it >= 0 && it < I_DM) ALOAD(wf[it], vo, rws, sW + ((it / 7) ^ rf) * 7168 + it % 7 * 1024);
    if constexpr (it >= I_DM && it < I_WD) ADMA(lb + 2 * UQ + (it - I_DM) * 1024, vo, rws, sU + 2 * UQ + (it - I_DM) * 1024);
    if constexpr (it >= I_WD && it < I_H0) {
      constexpr int k = it - I_WD;
      if constexpr (st_of(k) < 0) ALOAD(wd[k], vo, rws, sD + ((k / 7) ^ rd) * 1024 + k % 7 * 4096);
      else ADMA(l0 + stage(w, st_of(k)), vo, rws, sD + ((k / 7) ^ rd) * 1024 + k % 7 * 4096);
    }
    if constexpr (it >= I_H0 && it < I_END) ALOADC(hh[(it - I_H0) / 7][(it - I_H0) % 7], vH, rws, (sH ^ ((it - I_H0) % 7 & 1 ? HX : 0)) + (it - I_H0) / 7 * (112 * 1024) + (it - I_H0) % 7 * 2048);
    if constexpr (it >= I_PB && it < I_PB + 4) publish(it - I_PB);
    if constexpr (it == I_FLAG || it == I_FLAG + 1) flag(it - I_FLAG);
    if constexpr (it == I_POLL || it == I_POLL + 1) pollh(it - I_POLL);
    if constexpr (it == I_POLLR) pollr();
    if constexpr (it >= I_MK && it < I_MK + 2) BST((u32x4{SENT, SENT, SENT, SENT}), rws, vo, sP + (2 + it - I_MK) * (112 * 1024), AUX_H);
  };
  auto unstage = [&](auto J) { constexpr int j = J; wd[j + 16] = *(const u32x4*)(lds + stage(w, j) + vo); };
#pragma unroll
  for (int i = 0; i < PB - 1; ++i) bf[i] = trpair(lds + bo[0] + i * 1024, lds + bo[0] + i * 1024 + 256);
  sfor<0, 112>([&](auto I) {
    constexpr int i = I, u = i % 56, q = step_q(i), ks = step_k(i);
    if constexpr (i == SB2) { ST(4); VMW(SC.cnt[i] - SC.pos[I_WD - 1] - 1); BAR(); ST(5); }
    if constexpr (i == SB1) BAR();
    if constexpr (i == SB3) { STF(11); BAR(); ST(6); }
    if constexpr (i + PB - 1 < 112) {
      constexpr int n = i + PB - 1, off = (step_q(n) & 1) * UQ + step_k(n) * 1024;
      bf[n % PB] = trpair(lds + bo[n / 56] + off, lds + bo[n / 56] + off + 256);
    }
    constexpr int v = i - V1;
    if constexpr (v >= 0 && v < 28 && v % 4 == 0) vf[(v / 4) & 1] = trpair(lds + va + 2 * UQ + v * 256, lds + va + 2 * UQ + v * 256 + 256);
    if constexpr (q == 0) VMWT(SC.cnt[i] - SC.pos[ks] - 1, wf[ks]);
    if constexpr (i > 0 && prio(i) != prio(i - 1)) __builtin_amdgcn_s_setprio(prio(i));
    if constexpr (SC.at[i] == I_H0 || SC.at[i] == I_H1) {
      constexpr int fq = SC.at[i] == I_H1;
      if constexpr (!fq) { STF(12); }
      TIE(acc[0]); TIE(acc[1]); VMWT(SC.cnt[i] - SC.pos[I_POLL + fq] - 1, pf); flagwait(fq);
      if constexpr (!fq) { STF(13); }
      if constexpr (fq) { ST(7); }
    }
    if constexpr (ks == 0) {
      const f32x16 z = {0.f, 0.f, 0.f, 0.f, 0.f, 0.f, 0.f, 0.f, 0.f, 0.f, 0.f, 0.f, 0.f, 0.f, 0.f, 0.f};
      acc[q & 1] = mfma32(wf[ks], bf[i % PB], z);
    } else {
      acc[q & 1] = mfma32(wf[ks], bf[i % PB], acc[q & 1]);
    }
    if constexpr (v >= 3 && v < 31) { dot(vf[((v - 3) / 4) & 1][(v - 3) % 4], v & 1, v < 5); TIE(sq[v & 1]); }
    if constexpr (v == 31) sq_put(1);
    if constexpr (ks == 25) ps = *(const f32x4*)(lds + L_SMALL + q * 512 + ml * 16);
    if constexpr (ks == 27) { rs[q & 1] = __builtin_amdgcn_rsqf(((ps[0] + ps[1]) + (ps[2] + ps[3])) * (1.f / D) + 1e-5f); TIE(rs[q & 1]); }
    if constexpr (ks == 27) { c1[q & 1] = rs[q & 1] * -1.44269504088896f; }
    if constexpr (i >= G0 && i < G0 + 9) { glu(0, 2 * (i - G0)); glu(0, 2 * (i - G0) + 1); }
    if constexpr (i >= 57 && i < 75) glu(1, i - 57);
    if constexpr (i >= G2 && i < G2 + 18 / GD2) {
#pragma unroll
      for (int h = 0; h < GD2; ++h) glu(0, GD2 * (i - G2) + h);
    }
    if constexpr (i < 110 && (SC.at[i + 2] == I_FLAG || SC.at[i + 2] == I_FLAG + 1)) {
      constexpr int fq = SC.at[i + (i < 110 ? 2 : 0)] - I_FLAG;
      STF(1 + fq); VMW(SC.cnt[i] - SC.pos[I_PB + fq] - 1); STF(9 + fq);
      arr = __hip_atomic_fetch_add(cnt + fq * 64, 1u, __ATOMIC_RELAXED, __HIP_MEMORY_SCOPE_WORKGROUP);
    }
    issue(std::integral_constant<int, SC.at[i]>{});
    if constexpr (SC.cp[i] == 0) VMW(SC.cnt[i + 1] - SC.pos[I_H0 - 2] - 1);
    if constexpr (SC.cp[i] >= 0) unstage(std::integral_constant<int, SC.cp[i]>{});
    SCHED();
  });
  ST(8);
  sfor<SC.ncp, NS>(unstage);
  auto fetch = [&](int q, int i) { ALOADC(hh[q][i], vH, rws, (sH ^ (i & 1 ? HX : 0)) + q * (112 * 1024) + i * 2048); };
  auto check = [&](int q) {
#pragma unroll
    for (int i = 0; i < 7; ++i) TIE(hh[q][i]);
    for (int spin = 0; spin < (1 << 14); ++spin) {
      uint32_t m = 0;
#pragma unroll
      for (int i = 0; i < 7; ++i) m = max3u(max3u(m, hh[q][i][0], hh[q][i][1]), hh[q][i][2], hh[q][i][3]);
      if (__builtin_amdgcn_ballot_w64(m == SENT) == 0) break;
#pragma unroll
      for (int i = 0; i < 7; ++i) ARELOADC(hh[q][i], vH, rws, (sH ^ (i & 1 ? HX : 0)) + q * (112 * 1024) + i * 2048);
      VMW(0);
#pragma unroll
      for (int i = 0; i < 7; ++i) TIE(hh[q][i]);
    }
  };
  f32x4 a2[4][4];
  const f32x4 zero = {0.f, 0.f, 0.f, 0.f};
  auto gemm2 = [&](int q) {
#pragma unroll
    for (int i = 0; i < 7; ++i) {
#pragma unroll
      for (int cf = 0; cf < 4; ++cf) { a2[q][cf] = mfma16(wd[7 * cf + i], hh[q][i], i ? a2[q][cf] : zero); TIE(a2[q][cf]); }
      if (q == F2) fetch(2, i);
      if (q == F3) fetch(3, i);
      SCHED();
    }
  };
  auto spill = [&](int q) {
#pragma unroll
    for (int cf = 0; cf < 4; ++cf) *(f32x4*)(lds + ((q * 8 + w) * 16 + l15) * RED + (cf ^ rd) * 64 + g * 16) = a2[q][cf];
  };
  const int er = 2 * w + (lane >> 5), ec = 64 * ct + 2 * ml;
  uint32_t rc[4];
  const rsrc_t ro = mk(out, 2048 * 896);
  auto reduce = [&](int q) {
    f32x2 s = {lo16(rc[q]), hi16(rc[q])};
#pragma unroll
    for (int ww = 0; ww < 8; ++ww) s += *(const f32x2*)(lds + ((q * 8 + ww) * 16 + er) * RED + ml * 8);
    const int v = (int)pk(s[0], s[1]);
    const u32x4 ov = {(uint32_t)v, (uint32_t)__builtin_amdgcn_update_dpp(0, v, 0x55, 0xf, 0xf, true),
                      (uint32_t)__builtin_amdgcn_update_dpp(0, v, 0xaa, 0xf, 0xf, true),
                      (uint32_t)__builtin_amdgcn_update_dpp(0, v, 0xff, 0xf, 0xf, true)};
    BST(ov, ro, (lane & 3) == 0 ? er * (128 * 896) + ec * 2 : 0x80000000u, (8 * mt + 2 * q + rp) * 896, AUX_L);
  };
  __builtin_amdgcn_s_setprio(3);
#pragma unroll
  for (int i = 0; i < 18; ++i) glu(1, i);
  publish(3);
  __builtin_amdgcn_s_setprio(0);
  STT(9);
  asm volatile("s_waitcnt lgkmcnt(0)" ::: "memory");
  BAR();
  VMW(SC.pos[I_END] - SC.last0);
#pragma unroll
  for (int i = 0; i < 28; ++i)
    if (st_of(i) < 0) TIE(wd[i]);
#pragma unroll
  for (int i = 0; i < 7; ++i) TIE(hh[0][i]);
  gemm2(0);
  spill(0);
  VMWT(SC.pos[I_END] - SC.pos[I_END - 1] + (F2 == 0 ? 7 : 0), pr);
#pragma unroll
  for (int i = 0; i < 7; ++i) TIE(hh[1][i]);
  for (int spin = 0; __builtin_amdgcn_ballot_w64(pr == 0) != 0 && spin < (1 << 14); ++spin) {
    __builtin_amdgcn_s_sleep(1);
    pollr();
    VMWT(0, pr);
  }
  {
    const uint32_t vR = (ml >> 4) * 8192 + ((ml >> 3) & 1) * 1024 + (((ml >> 1) & 1) * 32 + rp * 16 + er) * 16 + ((ml >> 2) & 1) * 8 + (ml & 1) * 4;
#pragma unroll
    for (int q = 0; q < 4; ++q) ALOAD1C(rc[q], vR, rws, OFF_R + (mt * 14 + 2 * ct) * 8192 + q * 2048);
  }
  STT(1);
  gemm2(1);
  STT(2);
  spill(1);
  STT(10);
  SCHED();
  asm volatile("s_waitcnt vmcnt(%4)" : "+v"(rc[0]), "+v"(rc[1]), "+v"(rc[2]), "+v"(rc[3]) : "n"(F3 == 1 && F2 == 0 ? 7 : F3 == 1 ? 1 : 0) : "memory");
  check(2);
  STT(11);
  gemm2(2);
  spill(2);
  asm volatile("s_waitcnt lgkmcnt(0)" ::: "memory");
  BAR();
  STT(12);
  reduce(0); reduce(1); reduce(2);
  SCHED();
  VMW(3);
  check(3);
  STT(13);
  gemm2(3);
  spill(3);
  asm volatile("s_waitcnt lgkmcnt(0)" ::: "memory");
  BAR();
  reduce(3);
  asm volatile("k3_tail_a:");
  ST_END(ws, 3);
}

extern "C" void kernel_launch(void* const* d_in, const int* in_sizes, int n_in, void* d_out, int out_size,
                              void* d_ws, size_t ws_size, hipStream_t stream) {
  const uint16_t* x = (const uint16_t*)d_in[0];
  const uint16_t* wr = (const uint16_t*)d_in[1];
  const uint16_t* wi = (const uint16_t*)d_in[2];
  const uint16_t* wg = (const uint16_t*)d_in[4];
  const uint16_t* wu = (const uint16_t*)d_in[5];
  const uint16_t* wd = (const uint16_t*)d_in[6];
  char* ws = (char*)d_ws;
  k1<<<256, 512, 0, stream>>>(x, wr, wi, wg, wu, ws);
  k2<<<252, 512, 0, stream>>>(wg, wu, wd, ws);
  k3<<<256, 512, 0, stream>>>(ws, (uint16_t*)d_out);
}
```

```cpp
#include <hip/hip_runtime.h>
#include <stdint.h>
#include <type_traits>

typedef __attribute__((ext_vector_type(8))) __bf16 bf16x8;
typedef __attribute__((ext_vector_type(2))) __bf16 bf16x2;
typedef __attribute__((ext_vector_type(2))) float f32x2;
typedef __attribute__((ext_vector_type(4))) float f32x4;
typedef __attribute__((ext_vector_type(16))) float f32x16;
typedef __attribute__((ext_vector_type(4))) uint32_t u32x4;
typedef __attribute__((ext_vector_type(2))) uint32_t u32x2;
typedef __attribute__((ext_vector_type(4))) short s16x4;
typedef __amdgpu_buffer_rsrc_t rsrc_t;
#define LDSP __attribute__((address_space(3)))
#define DI __device__ __forceinline__
#define SCHED() __builtin_amdgcn_sched_barrier(0)
#define TIE(x) asm volatile("" : "+v"(x))

#define AUX_X 16
#define AUX_L 16

#define ST(i)
#define STF(i)
#define STT(i)
#define STC(i)
#define ST_END(ws, kid)
#define ST_BEGIN

DI f32x4 mfma16(u32x4 a, u32x4 b, f32x4 c) {
  return __builtin_amdgcn_mfma_f32_16x16x32_bf16(__builtin_bit_cast(bf16x8, a), __builtin_bit_cast(bf16x8, b), c, 0, 0, 0);
}
DI f32x16 mfma32(u32x4 a, u32x4 b, f32x16 c) {
  return __builtin_amdgcn_mfma_f32_32x32x16_bf16(__builtin_bit_cast(bf16x8, a), __builtin_bit_cast(bf16x8, b), c, 0, 0, 0);
}
DI uint32_t pk(float a, float b) {
  f32x2 v = {a, b};
  return __builtin_bit_cast(uint32_t, __builtin_convertvector(v, bf16x2));
}
DI float lo16(uint32_t v) { return __builtin_bit_cast(float, v << 16); }
DI float hi16(uint32_t v) { return __builtin_bit_cast(float, v & 0xffff0000u); }
DI float cosr(float r) { return __builtin_amdgcn_cosf(r); }
DI float sinr(float r) { return __builtin_amdgcn_sinf(r); }
template <int N> DI float ror16(float v) {
  return __builtin_bit_cast(float, __builtin_amdgcn_update_dpp(0, __builtin_bit_cast(int, v), 0x120 + N, 0xf, 0xf, true));
}
DI float sum32(float v) {
  v += ror16<8>(v); v += ror16<4>(v); v += ror16<2>(v); v += ror16<1>(v);
  const uint32_t u = __builtin_bit_cast(uint32_t, v);
  auto s = __builtin_amdgcn_permlane16_swap(u, u, false, false);
  return __builtin_bit_cast(float, (uint32_t)s[0]) + __builtin_bit_cast(float, (uint32_t)s[1]);
}
DI rsrc_t mk(const void* p, uint32_t n) { return __builtin_amdgcn_make_buffer_rsrc((void*)p, 0, n, 0x00020000); }
DI u32x4 bld(rsrc_t r, uint32_t voff, uint32_t soff) { return __builtin_amdgcn_raw_buffer_load_b128(r, voff, soff, 0); }
#define BST(v, r, voff, soff, aux) do { const u32x4 bv_ = (v); __builtin_amdgcn_raw_buffer_store_b128(bv_, r, voff, soff, aux); asm volatile("s_nop 1" ::"v"(bv_) : "memory"); } while (0)
#define BLDSA(r, ldsptr, voff, soff, aux) __builtin_amdgcn_raw_ptr_buffer_load_lds(r, (LDSP void*)(ldsptr), 16, voff, soff, 0, aux)
#define BLDS(r, ldsptr, voff, soff) BLDSA(r, ldsptr, voff, soff, 0)
#define NT 13
#define AUX_H 16
DI u32x4 trpair(const char* p0, const char* p1) {
  s16x4 a0 = __builtin_amdgcn_ds_read_tr16_b64_v4i16((LDSP s16x4*)p0);
  s16x4 a1 = __builtin_amdgcn_ds_read_tr16_b64_v4i16((LDSP s16x4*)p1);
  u32x2 b0 = __builtin_bit_cast(u32x2, a0), b1 = __builtin_bit_cast(u32x2, a1);
  return u32x4{b0[0], b0[1], b1[0], b1[1]};
}

template <int I, int N, class Fn> DI void sfor(Fn&& f) {
  if constexpr (I < N) { f(std::integral_constant<int, I>{}); sfor<I + 1, N>(f); }
}

constexpr int D = 448, F = 1792;
constexpr uint32_t ZS = 28800;
constexpr uint32_t UQ = 28672;
constexpr uint32_t OFF_Z = 0;
constexpr uint32_t OFF_U = 26u << 20;
constexpr uint32_t OFF_MX = (8u << 20) - (256u << 10);
constexpr uint32_t OFF_FL = OFF_MX + 65536;
constexpr uint32_t OFF_EP = OFF_FL + 65536;
#define NREP 16
#define RSTR 4352
#define PS 2
#define WDL 24
#define PW 0
constexpr uint32_t OFF_WGU = 8u << 20;
constexpr uint32_t OFF_WD = 12u << 20;
constexpr uint32_t OFF_E = OFF_WD + 7 * 56 * 4096;
#define OVL 0
constexpr uint32_t OFF_HH = OVL ? 0 : 14u << 20;
constexpr uint32_t OFF_WF = OVL ? 128 * ZS : 22u << 20;
constexpr uint32_t OFF_R = 24u << 20;
constexpr uint32_t WS_BYTES = 28u << 20;
constexpr uint32_t NEG2 = 0x80008000u;
constexpr uint32_t SENT = 0xffffffffu;
constexpr float BETA = 0.06681531047810609f;
constexpr float RSQ2 = 0.7071067811865476f;
constexpr int GS = 2064;
#define ST_GRIDS {256, 252, 256}
#define ST_SPLITS {{128, 0, 0}, {112, 224, 0}, {224, 0, 0}}
#define CPF 1
#define VMW(N) asm volatile("s_waitcnt vmcnt(%0)" ::"n"(N) : "memory")
#define BAR() asm volatile("s_barrier" ::: "memory")
#define ALOAD(dst, voff, rs, soff) asm volatile("buffer_load_dwordx4 %0, %1, %2, %3 offen" : "=v"(dst) : "v"(voff), "s"(rs), "s"(soff) : "memory")
#define TOUCH(dst, voff, rs, soff) asm volatile("buffer_load_dword %0, %1, %2, %3 offen" : "=v"(dst) : "v"(voff), "s"(rs), "s"(soff) : "memory")
DI f32x2 cis(float r) { return f32x2{cosr(r), sinr(r)}; }
DI f32x2 cmul(f32x2 a, f32x2 b) { return __builtin_elementwise_fma(f32x2{a[1], a[0]}, f32x2{-b[1], b[1]}, a * b[0]); }

DI void dft28(int g, int l15, u32x4& Ec, u32x4& Es) {
  const float kf = (float)l15 * (1.f / 28.f);
  float cc[8], ss[8];
#pragma unroll
  for (int j = 0; j < 8; ++j) {
    const float a = (float)(4 * g + (j & 3) + 4 * (j & 4)) * kf;
    cc[j] = cosr(a); ss[j] = sinr(-a);
  }
#pragma unroll
  for (int q = 0; q < 4; ++q) { Ec[q] = pk(cc[2 * q], cc[2 * q + 1]); Es[q] = pk(ss[2 * q], ss[2 * q + 1]); }
  const uint32_t m3 = g == 3 ? 0u : ~0u;
  Ec[2] &= m3; Ec[3] &= m3; Es[2] &= m3; Es[3] &= m3;
}
template <int STEP> DI void dft16(int d0, int l15, float (&tc)[4], float (&ts)[4], u32x4& Tr, u32x4& Ti) {
  float c[4], s[4];
#pragma unroll
  for (int j = 0; j < 4; ++j) {
    const float a = (float)(d0 + STEP * j) * (float)l15;
    tc[j] = cosr(a * (1.f / 448.f)); ts[j] = sinr(a * (1.f / 448.f));
    c[j] = BETA * cosr(a * (1.f / 16.f)); s[j] = BETA * sinr(a * (1.f / 16.f));
  }
  Tr[0] = pk(c[0], c[1]); Tr[1] = pk(c[2], c[3]); Tr[2] = pk(s[0], s[1]); Tr[3] = pk(s[2], s[3]);
  Ti[0] = Tr[2] ^ NEG2; Ti[1] = Tr[3] ^ NEG2; Ti[2] = Tr[0]; Ti[3] = Tr[1];
}
DI int slot_group(int g, int l15) {
  return (l15 == 15 || (l15 == 14 && g >= 2) || (l15 == 0 && g < 2)) ? 14 : (l15 == 14 ? 0 : l15);
}

DI void p1(const uint16_t* __restrict__ x, const uint16_t* __restrict__ wr, const uint16_t* __restrict__ wi,
           char* __restrict__ ws, char* lds) {
  constexpr int RS = 912;
  ST_BEGIN;
  const int t = threadIdx.x, lane = t & 63, l15 = lane & 15, g = lane >> 4;
  const int w = __builtin_amdgcn_readfirstlane(t >> 6);
  const rsrc_t rws = mk(ws, WS_BYTES);
  if (blockIdx.x >= 128) {
    const int u = blockIdx.x - 128, kq = u & 3, gu = (u >> 2) & 3, tt = (u >> 4) & 1, rho = u >> 5;
    const rsrc_t rr = mk(wr, 2048 * 225 * 2), ri = mk(wi, 2048 * 225 * 2);
    {
      const int n = 2 * w + (lane >> 5);
      const int k = 4 * kq + (n & 3) + 16 * rho + 64 * (16 * tt + 4 * gu + (n >> 2));
      const uint32_t o0 = ((uint32_t)(k * 450) & ~15u) + (lane & 31) * 16;
      const uint32_t o1 = ((uint32_t)(((2048 - k) & 2047) * 450) & ~15u) + (lane & 31) * 16;
      BLDSA(rr, lds + w * 1024, o0, 0, NT & 2);
      BLDSA(rr, lds + 8192 + w * 1024, o1, 0, NT & 2);
      BLDSA(ri, lds + 16384 + w * 1024, o0, 0, NT & 2);
      BLDSA(ri, lds + 24576 + w * 1024, o1, 0, NT & 2);
    }
    const int id = (w & 3) * 128 + u;
    uint32_t pf = 0;
    {
      uint32_t lo, hi, n;
      asm volatile("s_getpc_b64 s[20:21]\n\ts_add_u32 %0, s20, _Z2k1PKtS0_S0_S0_S0_S0_Pc@rel32@lo+4\n\ts_addc_u32 %1, s21, _Z2k1PKtS0_S0_S0_S0_S0_Pc@rel32@hi+12\n\t"
                   "s_max_i32 %2, 0, k3_tail_a-_Z2k1PKtS0_S0_S0_S0_S0_Pc\n\ts_max_i32 %2, %2, k3_tail_b-_Z2k1PKtS0_S0_S0_S0_S0_Pc" : "=s"(lo), "=s"(hi), "=s"(n)::"s20", "s21", "scc");
      TOUCH(pf, lane * 128, mk((const void*)(((uint64_t)hi << 32) | lo), CPF && u < 16 ? n : 0u), w * 8192);
    }
    u32x4 eo;
    if (w >= 4) {
      const int ks = id % 28, d = 32 * (id / 28) + (lane & 31);
      const int kd1 = ks >= 2 ? ks >> 1 : (ks ? 0 : 14), e = 8 * (ks & 1) + 4 * (lane >> 5);
      const float a0 = (float)(kd1 * d) * (1.f / 448.f);
#pragma unroll
      for (int r = 0; r < 4; ++r) {
        const float a = a0 + (float)(((e + r) * d) & 15) * (1.f / 16.f);
        const float sc = (r == 0 && ks == 1 && lane < 32) ? BETA * RSQ2 : BETA;
        eo[r] = pk(sc * cosr(a), -sc * sinr(a));
      }
      TIE(eo);
    }
    const int i = lane & 3, e = lane >> 2;
    const int a0 = ((4 * kq + i) * 450) & 15, a1 = (16 - a0) & 15;
    ST(1);
    VMW(1);
    BAR();
    ST(2);
    if (w >= 4) {
      if (id < 392) BST(eo, rws, lane * 16, OFF_E + id * 1024, AUX_X);
      if (w == 4) BST((u32x4{0u, 0u, 0u, 0u}), rws, lane < 16 && u < 112 ? (lane >> 2) * 128 + (lane & 3) * 16 : 0x80000000u, OFF_FL + u * 512, AUX_X);
    }
#pragma unroll
    for (int n = 0; n < 2; ++n) {
      const int G = w + 8 * n;
      if (G < 14) {
        const int kd = (G ? G : (e < 8 ? 14 : 0)) + 28 * e;
        const bool mir = kd > 224;
        const char* p = lds + (mir ? 8192 + a1 + 2 * (448 - kd) : a0 + 2 * kd) + i * 512;
        const uint32_t sg = mir ? 0x80000000u : 0u;
        u32x4 o;
#pragma unroll
        for (int r = 0; r < 4; ++r) {
          const uint32_t re = *(const uint16_t*)(p + r * 2048), im = *(const uint16_t*)(p + 16384 + r * 2048);
          o[r] = (re | (im << 16)) ^ sg;
        }
        BST(o, rws, e * 8192 + i * 16, OFF_WF + G * (16 * 8192) + (rho * 2 + tt) * 1024 + (16 * gu + 4 * kq) * 16, AUX_X);
      }
    }
    ST(3);
    asm volatile("s_waitcnt vmcnt(0)" ::"v"(pf) : "memory");
    ST_END(ws, 1);
    return;
  }
  const int s2 = blockIdx.x;
  const int s1 = t >> 5, c = t & 31;
  const bool ok1 = c < 24;
  const rsrc_t rx = mk(x, 2048 * 896);
  u32x4 v0, v1;
  v0 = __builtin_amdgcn_raw_buffer_load_b128(rx, s1 * (128 * 896) + c * 16, s2 * 896, (NT & 1) * 2);
  v1 = __builtin_amdgcn_raw_buffer_load_b128(rx, s1 * (128 * 896) + (ok1 ? c + 32 : c) * 16, s2 * 896, (NT & 1) * 2);
  SCHED();
  u32x4 Ec, Es, Tr, Ti;
  float tc[4], ts[4];
  dft28(g, l15, Ec, Es);
  dft16<1>(4 * g, l15, tc, ts, Tr, Ti);
  uint32_t b0 = 2 * w * RS + lane * 8, b1 = b0 + ((g == 3) ? 0 : 512);
  uint32_t wa0 = s1 * RS + c * 16, wa1 = s1 * RS + (ok1 ? c * 16 + 512 : 896);
  uint32_t zw = 16 * RS + slot_group(g, l15) * GS + g * 512 + w * 16;
  uint32_t zr_ = 16 * RS + (w >> 1) * GS + (w & 1) * 1024 + (lane >> 3) * 128 + (lane & 3) * 32 + ((lane >> 2) & 1) * 8;
  uint32_t mo = c == 0 ? s1 * 4 : 0x80000000u;
  float k1m = ok1 ? 1.f : 0.f, m28 = l15 == 0 ? -28.f : 0.f, nyq = lane == 32 ? RSQ2 : 1.f;
#pragma unroll
  for (int i = 0; i < 4; ++i) { TIE(tc[i]); TIE(ts[i]); }
  TIE(Ec); TIE(Es); TIE(Tr); TIE(Ti); TIE(b0); TIE(b1); TIE(wa0); TIE(wa1); TIE(zw); TIE(zr_); TIE(mo); TIE(k1m); TIE(m28); TIE(nyq);
  SCHED();
  *(u32x4*)(lds + wa0) = v0;
  *(u32x4*)(lds + wa1) = v1;
  ST(1);
  const f32x4 zero = {0.f, 0.f, 0.f, 0.f};
  f32x4 xr[2], xi[2];
#pragma unroll
  for (int n = 0; n < 2; ++n) {
    const u32x4 a = trpair(lds + b0 + n * RS, lds + b1 + n * RS);
    xr[n] = mfma16(a, Ec, zero);
    xi[n] = mfma16(a, Es, zero);
  }
  float mean, rstd;
  {
    const bf16x2 one = {(__bf16)1.f, (__bf16)1.f};
    float a0 = 0.f, a1 = 0.f, q0 = 0.f, q1 = 0.f;
#pragma unroll
    for (int i = 0; i < 4; ++i) {
      const uint32_t u0 = v0[i], u1 = v1[i];
      const bf16x2 e0 = __builtin_bit_cast(bf16x2, u0), e1 = __builtin_bit_cast(bf16x2, u1);
      a0 = __builtin_amdgcn_fdot2_f32_bf16(e0, one, a0, false); a1 = __builtin_amdgcn_fdot2_f32_bf16(e1, one, a1, false);
      q0 = __builtin_amdgcn_fdot2_f32_bf16(e0, e0, q0, false); q1 = __builtin_amdgcn_fdot2_f32_bf16(e1, e1, q1, false);
    }
    mean = sum32(__builtin_fmaf(a1, k1m, a0)) * (1.f / D);
    const float var = __builtin_fmaxf(sum32(__builtin_fmaf(q1, k1m, q0)) * (1.f / D) - mean * mean, 0.f);
    rstd = __builtin_amdgcn_rsqf(var + 1e-5f);
    __builtin_amdgcn_raw_buffer_store_b32(__builtin_bit_cast(uint32_t, mean), rws, mo, OFF_MX + s2 * 64, AUX_X);
  }
  f32x4 zr[2], zi[2];
  float rs[2];
#pragma unroll
  for (int n = 0; n < 2; ++n) {
    const float mn = __builtin_bit_cast(float, __builtin_amdgcn_readlane(__builtin_bit_cast(int, mean), 32 * n)) * m28;
    rs[n] = __builtin_bit_cast(float, __builtin_amdgcn_readlane(__builtin_bit_cast(int, rstd), 32 * n));
    float tr[4], ti[4];
#pragma unroll
    for (int r = 0; r < 4; ++r) {
      tr[r] = __builtin_fmaf(xi[n][r], ts[r], __builtin_fmaf(xr[n][r], tc[r], mn));
      ti[r] = __builtin_fmaf(xi[n][r], tc[r], -(xr[n][r] * ts[r]));
    }
    const u32x4 b = {pk(tr[0], tr[1]), pk(tr[2], tr[3]), pk(ti[0], ti[1]), pk(ti[2], ti[3])};
    zr[n] = mfma16(Tr, b, zero);
    zi[n] = mfma16(Ti, b, zero);
  }
  zr[0][0] *= nyq; zr[1][0] *= nyq;
#pragma unroll
  for (int r = 0; r < 4; ++r)
    *(u32x4*)(lds + zw + r * 128) = u32x4{pk(zr[0][r] * rs[0], zi[0][r] * rs[0]), pk(zr[1][r] * rs[1], zi[1][r] * rs[1]),
                                          pk(zr[0][r], zi[0][r]), pk(zr[1][r], zi[1][r])};
  ST(2);
  asm volatile("s_waitcnt lgkmcnt(0)" ::: "memory");
  BAR();
  ST(3);
  u32x2 o0[4], o1[4];
#pragma unroll
  for (int i = 0; i < 4; ++i) {
    const int ii = i < 3 || w < 4 ? i : 2;
    o0[i] = *(const u32x2*)(lds + zr_ + ii * (4 * GS)); o1[i] = *(const u32x2*)(lds + zr_ + ii * (4 * GS) + 16);
  }
#pragma unroll
  for (int i = 0; i < 4; ++i)
    if (i < 3 || w < 4) BST((u32x4{o0[i][0], o0[i][1], o1[i][0], o1[i][1]}), rws, lane * 16, OFF_Z + s2 * ZS + (i * 8 + w) * 1024, AUX_X);
  ST_END(ws, 1);
}

template <int N> DI void rot_acc(f32x4& tr, f32x4& ti, u32x4 d, u32x4 P, u32x4 Q, u32x4 nP, u32x4 nQ) {
  const u32x4 cr = (N == 0) ? P : (N == 1) ? Q : (N == 2) ? nP : nQ;
  const u32x4 ci = (N == 0) ? Q : (N == 1) ? nP : (N == 2) ? nQ : P;
  tr = mfma16(cr, d, tr); ti = mfma16(ci, d, ti);
}
template <int KS> DI void k2_inv(f32x4 (&ur)[2], f32x4 (&ui)[2], const u32x4 (&rf)[8], u32x4 P, u32x4 Q) {
  const u32x4 nP = P ^ NEG2, nQ = Q ^ NEG2;
#pragma unroll
  for (int tb = 0; tb < 2; ++tb) rot_acc<0>(ur[tb], ui[tb], rf[tb], P, Q, nP, nQ);
#pragma unroll
  for (int tb = 0; tb < 2; ++tb) rot_acc<(4 - (KS & 3)) & 3>(ur[tb], ui[tb], rf[2 + tb], P, Q, nP, nQ);
#pragma unroll
  for (int tb = 0; tb < 2; ++tb) rot_acc<(4 - ((2 * KS) & 3)) & 3>(ur[tb], ui[tb], rf[4 + tb], P, Q, nP, nQ);
#pragma unroll
  for (int tb = 0; tb < 2; ++tb) rot_acc<(4 - ((3 * KS) & 3)) & 3>(ur[tb], ui[tb], rf[6 + tb], P, Q, nP, nQ);
}

DI void p2(const uint16_t* __restrict__ wg, const uint16_t* __restrict__ wu, const uint16_t* __restrict__ wd,
           char* __restrict__ ws, char* lds, uint32_t tag) {
  ST_BEGIN;
  const int t = threadIdx.x, lane = t & 63, l15 = lane & 15, g = lane >> 4;
  const int w8 = __builtin_amdgcn_readfirstlane(t >> 6), w = w8 & 3, hf = w8 >> 2;
  const rsrc_t rws = mk(ws, WS_BYTES);
  const f32x4 zero = {0.f, 0.f, 0.f, 0.f};
  const int q = (lane >> 2) & 3, p = lane & 3;
  if (blockIdx.x >= 224) {
    const int kb = blockIdx.x - 224;
    const rsrc_t rd = mk(wd, D * F * 2);
#pragma unroll
    for (int i = 0; i < 7; ++i) BLDSA(rd, lds + (i * 8 + w8) * 1024, lane * 16, kb * 57344 + (i * 8 + w8) * 1024, (NT >> 2) & 2);
    const char* b = lds + hf * 28672 + (16 * (g >> 1) + 4 * (g & 1) + q) * 896 + p * 8 + w * 32;
    u32x4 o[7];
    ST(1);
    __syncthreads();
    ST(2);
#pragma unroll
    for (int i = 0; i < 7; ++i) o[i] = trpair(b + i * 128, b + i * 128 + 8 * 896);
#pragma unroll
    for (int i = 0; i < 7; ++i) BST(o[i], rws, lane * 16, OFF_WD + ((i * 56 + 2 * kb + hf) * 4 + w) * 1024, AUX_X);
    ST_END(ws, 2);
    return;
  }
  if (blockIdx.x >= 112) {
    const int u = blockIdx.x - 112, pp = (u >> 4) * 8 + (u & 7), mat = pp & 1, fb = (pp & ~1) + ((u >> 3) & 1);
    const rsrc_t rw = mk(mat ? wu : wg, D * F * 2);
    const uint32_t vo = (lane >> 2) * (F * 2) + (((lane & 3) ^ w) << 4);
#pragma unroll
    for (int i = 0; i < 4; ++i)
      if (i < 3 || w8 < 4) BLDSA(rw, lds + (w8 + 8 * i) * 1024, vo, fb * 64 + (w8 + 8 * i) * (16 * F * 2), (NT >> 1) & 2);
    u32x4 Ec, Es, Tr, Ti;
    float tc[4], ts[4];
    dft28(g, l15, Ec, Es);
    dft16<4>(g, l15, tc, ts, Tr, Ti);
    uint32_t b0 = (4 * g + q) * 1024 + p * 64 + ((w ^ q) << 4) + hf * 8, b1 = b0 + (g == 3 ? 0 : 16384);
    uint32_t zw = slot_group(g, l15) * GS + g * 512 + w * 128 + hf * 64 + 28672;
    float nyq = lane == 32 ? RSQ2 : 1.f;
#pragma unroll
    for (int i = 0; i < 4; ++i) { TIE(tc[i]); TIE(ts[i]); }
    TIE(Ec); TIE(Es); TIE(Tr); TIE(Ti); TIE(b0); TIE(b1); TIE(zw); TIE(nyq);
    ST(1);
    __syncthreads();
    ST(2);
    u32x4 a[4];
#pragma unroll
    for (int tt = 0; tt < 4; ++tt) a[tt] = trpair(lds + b0 + tt * 256, lds + b1 + tt * 256);
    {
      f32x4 xr[4], xi[4];
#pragma unroll
      for (int tt = 0; tt < 4; ++tt) { xr[tt] = mfma16(a[tt], Ec, zero); xi[tt] = mfma16(a[tt], Es, zero); }
#pragma unroll
      for (int r = 0; r < 4; ++r) {
        float tr[4], ti[4];
#pragma unroll
        for (int tt = 0; tt < 4; ++tt) {
          tr[tt] = __builtin_fmaf(xi[tt][r], ts[tt], xr[tt][r] * tc[tt]);
          ti[tt] = __builtin_fmaf(xi[tt][r], tc[tt], -(xr[tt][r] * ts[tt]));
        }
        const u32x4 b = {pk(tr[0], tr[1]), pk(tr[2], tr[3]), pk(ti[0], ti[1]), pk(ti[2], ti[3])};
        const f32x4 zr = mfma16(Tr, b, zero), zi = mfma16(Ti, b, zero);
        *(u32x4*)(lds + zw + r * 16) = u32x4{pk(zr[0] * nyq, zi[0]), pk(zr[1], zi[1]), pk(zr[2], zi[2]), pk(zr[3], zi[3])};
      }
    }
    ST(3);
    __syncthreads();
    ST(4);
    const uint32_t so = ((lane >> 4) & 1) * 28672 + (lane >> 5) * 512 + l15 * 16;
#pragma unroll
    for (int i = 0; i < 4; ++i) a[i] = *(const u32x4*)(lds + 28672 + (w8 >> 1) * GS + (w8 & 1) * 1024 + lane * 16 + (i < 3 || w8 < 4 ? i : 2) * (4 * GS));
#pragma unroll
    for (int i = 0; i < 4; ++i)
      if (i < 3 || w8 < 4) BST(a[i], rws, so, OFF_WGU + fb * 57344 + mat * 256 + (w8 + 8 * i) * 1024, AUX_X);
    ST_END(ws, 2);
    return;
  }
  const int slot = (blockIdx.x & 7) * 28 + 2 * (blockIdx.x >> 3) + hf;
  char* const lh = lds + hf * 24576;
  u32x4 zf[8], wf[2], zx[2];
  u32x4 Br[4], Bi[4], Ar, Ai, C1[2], S1[2];
  f32x4 fc[2], fs[2], ic[2], is[2];
  {
    const float lf = (float)l15, g4 = (float)(4 * g), wf_ = (float)w;
    const float kc = lf + 16.f * wf_;
    {
      const f32x2 st = cis(kc * (1.f / 64.f));
      f32x2 z = cis(g4 * (kc * (1.f / 16.f)));
#pragma unroll
      for (int n = 0; n < 16; ++n) {
        Br[n & 3][n >> 2] = pk(z[0], z[1]); Bi[n & 3][n >> 2] = pk(-z[1], z[0]);
        z = cmul(z, st);
      }
    }
    {
      const f32x2 st = cis(kc * (1.f / 2048.f)), jp = cis(kc * (1.f / 128.f));
      f32x2 z = cis(g4 * (kc * (1.f / 2048.f)));
#pragma unroll
      for (int j = 0; j < 4; ++j) {
        const f32x2 y = cmul(z, jp);
        fc[0][j] = z[0]; fs[0][j] = z[1]; fc[1][j] = y[0]; fs[1][j] = y[1];
        z = cmul(z, st);
      }
    }
    const uint32_t lsg = (l15 & 1) ? NEG2 : 0u;
    {
      const f32x2 st = cis(lf * (1.f / 32.f)), z0 = cis(g4 * (lf * (1.f / 32.f))), z1 = cmul(z0, st), z2 = cmul(z1, st), z3 = cmul(z2, st);
      C1[0][0] = pk(z0[0], z1[0]); C1[0][1] = pk(z2[0], z3[0]); S1[0][0] = pk(z0[1], z1[1]); S1[0][1] = pk(z2[1], z3[1]);
#pragma unroll
      for (int q2 = 0; q2 < 2; ++q2) { C1[0][q2 + 2] = C1[0][q2] ^ lsg; S1[0][q2 + 2] = S1[0][q2] ^ lsg; }
    }
    C1[1] = C1[0] ^ 0x80000000u; S1[1] = S1[0] ^ 0x80000000u;
#pragma unroll
    for (int h = 0; h < 2; ++h) {
      const float b = (lf + (float)(16 * h)) * (1.f / 2048.f);
      const f32x2 st = cis(b);
      f32x2 z = cis((g4 + 16.f * wf_) * b) * (1.f / 2048.f);
#pragma unroll
      for (int j = 0; j < 4; ++j) { ic[h][j] = z[0]; is[h][j] = z[1]; z = cmul(z, st); }
    }
    {
      const float b = (4.f * lf + wf_) * (1.f / 64.f);
      const f32x2 st = cis(b), z0 = cis(g4 * b), z1 = cmul(z0, st), z2 = cmul(z1, st), z3 = cmul(z2, st);
      Ar[0] = pk(z0[0], z1[0]); Ar[1] = pk(z2[0], z3[0]);
      Ai[0] = pk(z0[1], z1[1]); Ai[1] = pk(z2[1], z3[1]);
    }
    Ar[2] = Ai[0] ^ NEG2; Ar[3] = Ai[1] ^ NEG2; Ai[2] = Ar[0]; Ai[3] = Ar[1];
#pragma unroll
    for (int j = 0; j < 2; ++j) { TIE(fc[j]); TIE(fs[j]); TIE(ic[j]); TIE(is[j]); }
    TIE(Ar); TIE(Ai);
  }
  uint32_t za = hf * 24576 + 8192 + l15 * 128 + ((g ^ (l15 >> 1)) << 4);
  TIE(za);
#pragma unroll
  for (int j = 0; j < 4; ++j) { TIE(Br[j]); TIE(Bi[j]); }
  TIE(C1[0]); TIE(S1[0]);
  SCHED();
  ST(5);
  if (w8 == PW) {
    for (int it = 0; it < (1 << 22); ++it) {
      u32x4 f;
      asm volatile("buffer_load_dwordx4 %0, %1, %2, %3 offen sc1\n\ts_waitcnt vmcnt(0)" : "=v"(f) : "v"(lane * 16), "s"(rws), "s"(OFF_EP + (blockIdx.x % NREP) * RSTR) : "memory");
      if (__builtin_amdgcn_ballot_w64(f[0] != tag || f[1] != tag || f[2] != tag || f[3] != tag) == 0) break;
      if (PS) __builtin_amdgcn_s_sleep(PS);
    }
    if (blockIdx.x == 0 && lane == 0) __builtin_amdgcn_raw_buffer_store_b32(tag, rws, 0, OFF_EP + 98304, 16);
  }
  BAR();
  ST(6);
  {
    const uint32_t so = OFF_Z + slot * 128 + w * (32 * ZS), vz = (lane >> 3) * ZS + (((lane & 7) ^ (lane >> 4)) << 4);
#pragma unroll
    for (int i = 0; i < 4; ++i) BLDSA(rws, lh + 8192 + (4 * w + i) * 1024, vz ^ ((i & 1) << 6), so + i * (8 * ZS), 16);
    wf[0] = __builtin_amdgcn_raw_buffer_load_b128(rws, lane * 16, OFF_WF + (slot * 4 + w) * 2048, 16);
    wf[1] = __builtin_amdgcn_raw_buffer_load_b128(rws, lane * 16, OFF_WF + (slot * 4 + w) * 2048 + 1024, 16);
  }
  SCHED();
  ST(3);
  VMW(2);
  BAR();
  ST(4);
#pragma unroll
  for (int i = 0; i < 8; ++i) zf[i] = *(const u32x4*)(lds + za + i * 2048);
#pragma unroll
  for (int i = 0; i < 2; ++i) zx[i] = *(const u32x4*)(lds + (za ^ 64) + (2 * w + i) * 2048);
  SCHED();
  f32x4 tr[2] = {zero, zero}, ti[2] = {zero, zero};
#pragma unroll
  for (int a = 0; a < 4; ++a)
#pragma unroll
    for (int tb = 0; tb < 2; ++tb) { tr[tb] = mfma16(zf[2 * a + tb], Br[a], tr[tb]); ti[tb] = mfma16(zf[2 * a + tb], Bi[a], ti[tb]); }
  {
    f32x4 pr[2], pi[2];
#pragma unroll
    for (int j = 0; j < 2; ++j) {
      pr[j] = __builtin_elementwise_fma(ti[j], fs[j], tr[j] * fc[j]);
      pi[j] = __builtin_elementwise_fma(ti[j], fc[j], -(tr[j] * fs[j]));
    }
    const u32x4 Pr = {pk(pr[0][0], pr[0][1]), pk(pr[0][2], pr[0][3]), pk(pr[1][0], pr[1][1]), pk(pr[1][2], pr[1][3])};
    const u32x4 Pi = {pk(pi[0][0], pi[0][1]), pk(pi[0][2], pi[0][3]), pk(pi[1][0], pi[1][1]), pk(pi[1][2], pi[1][3])};
    const u32x4 Nr = Pr ^ NEG2;
    float yr[8], yi[8];
#pragma unroll
    for (int tt = 0; tt < 2; ++tt) {
      f32x4 xr = mfma16(C1[tt], Pr, zero), xi = mfma16(C1[tt], Pi, zero);
      xr = mfma16(S1[tt], Pi, xr); xi = mfma16(S1[tt], Nr, xi);
#pragma unroll
      for (int r = 0; r < 4; ++r) {
        const float a = lo16(wf[tt][r]), b = hi16(wf[tt][r]);
        yr[4 * tt + r] = xr[r] * a - xi[r] * b;
        yi[4 * tt + r] = xr[r] * b + xi[r] * a;
      }
    }
    const u32x4 Yr = {pk(yr[0], yr[1]), pk(yr[2], yr[3]), pk(yr[4], yr[5]), pk(yr[6], yr[7])};
    const u32x4 Yi = {pk(yi[0], yi[1]), pk(yi[2], yi[3]), pk(yi[4], yi[5]), pk(yi[6], yi[7])};
    const u32x4 Ni = Yi ^ NEG2;
#pragma unroll
    for (int tb = 0; tb < 2; ++tb) {
      f32x4 rr = mfma16(Yr, C1[tb], zero), ri = mfma16(Yr, S1[tb], zero);
      rr = mfma16(Ni, S1[tb], rr); ri = mfma16(Yi, C1[tb], ri);
      const f32x4 vr = __builtin_elementwise_fma(rr, ic[tb], -(ri * is[tb])), vi = __builtin_elementwise_fma(ri, ic[tb], rr * is[tb]);
      *(u32x4*)(lh + (w * 2 + tb) * 1024 + lane * 16) = u32x4{pk(vr[0], vr[1]), pk(vr[2], vr[3]), pk(vi[0], vi[1]), pk(vi[2], vi[3])};
    }
  }
  ST(1);
  __syncthreads();
  ST(2);
  u32x4 rf[8];
#pragma unroll
  for (int i = 0; i < 8; ++i) rf[i] = *(const u32x4*)(lh + i * 1024 + lane * 16);
  f32x4 ur[2], ui[2];
#pragma unroll
  for (int tb = 0; tb < 2; ++tb)
#pragma unroll
    for (int r = 0; r < 4; ++r) { ur[tb][r] = lo16(zx[tb][r]); ui[tb][r] = hi16(zx[tb][r]); }
  if (w == 0) k2_inv<0>(ur, ui, rf, Ar, Ai);
  else if (w == 1) k2_inv<1>(ur, ui, rf, Ar, Ai);
  else if (w == 2) k2_inv<2>(ur, ui, rf, Ar, Ai);
  else k2_inv<3>(ur, ui, rf, Ar, Ai);
  const uint32_t im = slot == 8 ? 0u : ~0u;
#pragma unroll
  for (int tb = 0; tb < 2; ++tb) {
    u32x4 o = {pk(ur[tb][0], ur[tb][1]), pk(ur[tb][2], ur[tb][3]), pk(ui[tb][0], ui[tb][1]) & im, pk(ui[tb][2], ui[tb][3]) & im};
    BST(o, rws, (l15 >> 1) * UQ + (l15 & 1) * 64 + g * 16, OFF_U + (16 * w + 8 * tb) * UQ + slot * 128, AUX_X);
  }
  ST_END(ws, 2);
}

__global__ __launch_bounds__(512, 2) void k1(const uint16_t* __restrict__ x, const uint16_t* __restrict__ wr,
                                             const uint16_t* __restrict__ wi, const uint16_t* __restrict__ wg,
                                             const uint16_t* __restrict__ wu, const uint16_t* __restrict__ wd, char* __restrict__ ws) {
  __shared__ __attribute__((aligned(16))) char lds[28672 + 15 * GS];
  const rsrc_t rws = mk(ws, WS_BYTES);
  const uint32_t tag = __builtin_amdgcn_raw_buffer_load_b32(rws, 0, OFF_EP + 98304, 16) * 0x9E3779B1u + 0x7F4A7C15u;
  p1(x, wr, wi, ws, lds);
  VMW(0);
  BAR();
  if (threadIdx.x < NREP) __builtin_amdgcn_raw_buffer_store_b32(tag, rws, threadIdx.x * RSTR, OFF_EP + blockIdx.x * 4, 16);
  if (WDL && blockIdx.x >= 112) __builtin_amdgcn_s_sleep(WDL);
  if (blockIdx.x < 252) p2(wg, wu, wd, ws, lds, tag);
}

#define ALOADC(dst, voff, rs, soff) asm volatile("buffer_load_dwordx4 %0, %1, %2, %3 offen sc1" : "=v"(dst) : "v"(voff), "s"(rs), "s"(soff) : "memory")
#define ALOAD1(dst, voff, rs, soff) asm volatile("buffer_load_dword %0, %1, %2, %3 offen" : "=v"(dst) : "v"(voff), "s"(rs), "s"(soff) : "memory")
#define ALOAD1C(dst, voff, rs, soff) asm volatile("buffer_load_dword %0, %1, %2, %3 offen sc1" : "=v"(dst) : "v"(voff), "s"(rs), "s"(soff) : "memory")
#define ADMA(ldsa, voff, rs, soff) asm volatile("s_mov_b32 m0, %0\n\ts_nop 0\n\tbuffer_load_dwordx4 %1, %2, %3 offen lds" ::"s"(ldsa), "v"(voff), "s"(rs), "s"(soff) : "memory")
#define VMWT(N, x) asm volatile("s_waitcnt vmcnt(%1)" : "+v"(x) : "n"(N) : "memory")
#define ARELOADC(dst, voff, rs, soff) asm volatile("buffer_load_dwordx4 %0, %1, %2, %3 offen sc1" : "+v"(dst) : "v"(voff), "s"(rs), "s"(soff) : "memory")
constexpr int RED = 272;
constexpr int L_SMALL = 32 * 16 * RED;
constexpr int L_CNT = L_SMALL + 2048 + 16384;
DI uint32_t umax(uint32_t a, uint32_t b) { return a > b ? a : b; }
DI uint32_t umin(uint32_t a, uint32_t b) { return a < b ? a : b; }
DI uint32_t max3u(uint32_t a, uint32_t b, uint32_t c) { return umax(umax(a, b), c); }

#define LAG1 12
#define LAG2 20
constexpr int step_q(int i) { const int u = i % 56, L = i < 56 ? LAG1 : LAG2; return 2 * (i / 56) + (u < L ? 0 : u >= 56 - L ? 1 : ((u - L) & 1) ^ 1); }
constexpr int step_k(int i) { const int u = i % 56, L = i < 56 ? LAG1 : LAG2; return u < L ? u : u >= 56 - L ? u - 28 : ((u - L) & 1) ? L + (u - L) / 2 : (u - L) / 2; }
#define NE 16
#define WE 13
#define R1M 3
#define R2M 3
#define SB3 87
#define FD 11
#define PD 8
#define CD 8
#define F2 0
#define F3 2
#define WD0 44
#define MK0 40
#define PR 99
#define ROT 0
#define GD2 2
constexpr int SB1 = 24, SB2 = 52, G0 = 57 - LAG1, G2 = 113 - LAG2, V1 = 53;
static_assert(SB3 <= 107 - LAG2 && SB3 > V1 + 32 && LAG1 >= 12 && LAG2 >= 20, "schedule");
constexpr int prio(int i) { return 3 - (i < SB1 ? i * 4 / SB1 : i < SB2 ? (i - SB1) * 4 / (SB2 - SB1) : i < SB3 ? (i - SB2) * 4 / (SB3 - SB2) : (i - SB3) * 4 / (112 - SB3)); }
constexpr int I_DM = 28, I_WD = 35, I_H0 = 63, I_H1 = 70, I_END = 77;
constexpr int I_PB = 90, I_FLAG = 94, I_POLL = 96, I_MK = 98, I_POLLR = 100;
constexpr int NS = 11, S_PB[2] = {G0 + 9, 75};
constexpr int st_of(int n) { return n >= 16 && n < 27 ? n - 16 : -1; }
constexpr int stage(int w, int j) { return j < 3 ? 4 * 28672 + (w * 3 + j) * 1024 : j < 5 ? L_SMALL + 2048 + (w * 2 + j - 3) * 1024 : (w * 6 + j - 5) * 1024; }
struct Sched { int at[112]; int cp[112]; int pos[101]; int cnt[113]; int next, ncp, last0; };
constexpr Sched make_sched() {
  Sched s{};
  int n = 0, nx = WE, nh = I_H0, used = 0, nc = 0;
  for (int k = 0; k < WE; ++k) s.pos[k] = n++;
  for (int i = 0; i < 112; ++i) {
    s.cnt[i] = n;
    int it = -1, room = NE - used;
    for (int j = 0; j <= i - 2; ++j) room += step_q(j) == 3;
    const int st = nx >= I_WD ? st_of(nx - I_WD) : -1;
    if (i == S_PB[0]) it = I_PB;
    else if (i == S_PB[1]) it = I_PB + 1;
    else if (i == G2 + 18 / GD2) it = I_PB + 2;
    else if (i == S_PB[0] + FD) it = I_FLAG;
    else if (i == S_PB[1] + FD) it = I_FLAG + 1;
    else if (i == S_PB[0] + FD + PD) it = I_POLL;
    else if (i == S_PB[1] + FD + PD) it = I_POLL + 1;
    else if (i == PR) it = I_POLLR;
    else if (i == MK0) it = I_MK;
    else if (i == MK0 + 2) it = I_MK + 1;
    else if (nx < I_WD) { if (i % R1M != R1M - 1) it = nx++; }
    else if (nh < I_END && nx >= I_WD + 16 && room > 0 && i >= S_PB[nh >= I_H1] + FD + PD + CD) { it = nh++; ++used; --room; }
    else if (nx < I_H0 && i >= WD0 && (i % R2M != R2M - 1 || i >= 76) && (st >= 0 ? (st < 5 || i > SB2) : room > 0)) {
      it = nx++;
      if (st < 0) { ++used; --room; }
    }
    s.at[i] = it;
    if (it >= 0) s.pos[it] = n++;
    s.cp[i] = -1;
    if (nx == I_H0 && nc < NS && room > 0 && (nh == I_END || 112 - i <= NS - nc) && i >= 96) { s.cp[i] = nc++; ++used; }
  }
  s.cnt[112] = n;
  s.next = nx == I_H0 ? nh : 0;
  s.ncp = nc;
  s.pos[I_END] = n;
  s.last0 = s.pos[I_H0 - 1] > s.pos[I_H1 - 1] ? s.pos[I_H0 - 1] : s.pos[I_H1 - 1];
  return s;
}
constexpr Sched SC = make_sched();
static_assert(SC.next == I_END && SC.ncp > 0 && SC.pos[I_POLLR] < SC.pos[I_END - 1] && SC.last0 < SC.pos[I_END - 1], "schedule");

__global__ __launch_bounds__(512, 2) void k3(char* __restrict__ ws, uint16_t* __restrict__ out) {
  __shared__ __attribute__((aligned(16))) char lds[L_CNT + 512];
  ST_BEGIN;
  const int t = threadIdx.x, lane = t & 63, l15 = lane & 15, g = lane >> 4;
  const int w = __builtin_amdgcn_readfirstlane(t >> 6);
  const int xcd = blockIdx.x & 7, jb = blockIdx.x >> 3;
  const int mt = 4 * (xcd >> 1) + (jb & 3), rp = xcd & 1, ct = jb >> 2, nt = 7 * rp + ct;
  const rsrc_t rws = mk(ws, WS_BYTES);
  const uint32_t vo = lane * 16;
  const int rf = (ROT & 1) && ct < 7 ? jb & 3 : 0, rd = (ROT & 2 ? jb & 3 : 0) ^ (ROT & 4 ? w & 3 : 0);
  const uint32_t lb = (uint32_t)(uintptr_t)(LDSP char*)lds + w * 7168, sU = OFF_U + mt * (4 * UQ) + (w ^ rf) * 7168;
#pragma unroll
  for (int n = 0; n < 7; ++n) ADMA(lb + n * 1024, vo, rws, sU + n * 1024);
  const int ml = lane & 31;
  uint32_t bo[2];
  bo[0] = (lane >> 5) * 512 + ((lane >> 3) & 1) * 128 + (g & 1) * 64 + (lane & 3) * 16 + ((lane >> 2) & 1) * 8;
  bo[1] = bo[0] + 2 * UQ;
  TIE(bo[0]); TIE(bo[1]);
  constexpr int PB = 4;
  if (ct == 7) {
    uint32_t mx[4];
#pragma unroll
    for (int q = 0; q < 4; ++q) ALOAD1(mx[q], ml * 4, rws, OFF_MX + (8 * mt + 2 * q) * 64);
    BAR();
    if (w == 7) {
      VMW(0);
      BAR();
    } else {
      const int j = 7 * rp + w;
      const uint32_t sE = OFF_E + j * (28 * 1024), sR = OFF_R + (mt * 14 + j) * 8192;
      constexpr int EE = 12;
      u32x4 e[28], bf[PB];
#pragma unroll
      for (int i = 0; i < EE; ++i) ALOAD(e[i], vo, rws, sE + i * 1024);
      asm volatile("s_waitcnt vmcnt(%4)" : "+v"(mx[0]), "+v"(mx[1]), "+v"(mx[2]), "+v"(mx[3]) : "n"(EE) : "memory");
      BAR();
      ST(1);
      f32x16 acc[4];
#pragma unroll
      for (int q = 0; q < 4; ++q)
#pragma unroll
        for (int r = 0; r < 16; ++r) acc[q][r] = __builtin_bit_cast(float, mx[q]);
      auto put = [&](int q) {
#pragma unroll
        for (int h = 0; h < 2; ++h)
          BST((u32x4{pk(acc[q][8 * h], acc[q][8 * h + 1]), pk(acc[q][8 * h + 2], acc[q][8 * h + 3]), pk(acc[q][8 * h + 4], acc[q][8 * h + 5]),
                     pk(acc[q][8 * h + 6], acc[q][8 * h + 7])}), rws, vo, sR + (q * 2 + h) * 1024, 16);
      };
#pragma unroll
      for (int i = 0; i < PB - 1; ++i) bf[i] = trpair(lds + bo[0] + (i & 1) * UQ + (i >> 1) * 1024, lds + bo[0] + (i & 1) * UQ + (i >> 1) * 1024 + 256);
      sfor<0, 112>([&](auto I) {
        constexpr int i = I, q = 2 * (i / 56) + (i & 1), ks = (i % 56) >> 1, NL = 28 - EE + 8;
        if constexpr (i == 52) { VMW(0); BAR(); }
        if constexpr (i + PB - 1 < 112) {
          constexpr int n = i + PB - 1, off = (n & 1) * UQ + ((n % 56) >> 1) * 1024;
          bf[n % PB] = trpair(lds + bo[n / 56] + off, lds + bo[n / 56] + off + 256);
        }
        if constexpr (q == 0) VMWT(EE + (i < NL ? i : NL) - ks - 1, e[ks]);
        acc[q] = mfma32(e[ks], bf[i % PB], acc[q]);
        if constexpr (i < 28 - EE) ALOAD(e[EE + i], vo, rws, sE + (EE + i) * 1024);
        else if constexpr (i < NL) ADMA(lb + 2 * UQ + (w + i - 28 + EE) * 1024, vo, rws, sU + 2 * UQ + (w + i - 28 + EE) * 1024);
        if constexpr (i == 60) put(0);
        if constexpr (i == 64) put(1);
        SCHED();
      });
      ST(2);
      put(2); put(3);
      asm volatile("k3_tail_b:");
      VMW(0);
      if (lane == 0) __builtin_amdgcn_raw_buffer_store_b32(1u, rws, 0, OFF_FL + (224 + mt * 14 + j) * 128, 16);
    }
    ST_END(ws, 3);
    return;
  }
  u32x4 wf[28];
  const uint32_t sW = OFF_WGU + (nt * 8 + w) * (28 * 1024);
  const uint32_t sP = OFF_HH + (mt * 4 * 112 + nt * 8 + w) * 1024;
  uint32_t va = bo[0] + (w & 1) * UQ + (w >> 1) * 7168;
  TIE(va);
  if (w < 2) *(uint32_t*)(lds + L_CNT + w * 256 + lane * 4) = 0u;
  BAR();
#pragma unroll
  for (int i = 0; i < WE - 7; ++i) ALOAD(wf[i], vo, rws, sW + ((i / 7) ^ rf) * 7168 + i % 7 * 1024);
  ST(1);
  VMW(WE - 7);
  ST(2);
  asm volatile("s_waitcnt lgkmcnt(0)" ::: "memory");
  BAR();
  float sq[2];
  auto dot = [&](uint32_t x, int a, bool first) {
    const bf16x2 e0 = __builtin_bit_cast(bf16x2, x);
    sq[a] = __builtin_amdgcn_fdot2_f32_bf16(e0, e0, first ? 0.f : sq[a], false);
  };
  auto sq_put = [&](int pair) {
    const uint32_t x0 = __builtin_bit_cast(uint32_t, sq[0] + sq[1]);
    auto s = __builtin_amdgcn_permlane32_swap(x0, x0, false, false);
    *(float*)(lds + L_SMALL + pair * 1024 + (w & 1) * 512 + ml * 16 + (w >> 1) * 4) =
        __builtin_bit_cast(float, (uint32_t)s[0]) + __builtin_bit_cast(float, (uint32_t)s[1]);
  };
  {
    u32x4 vf[7];
#pragma unroll
    for (int i = 0; i < 7; ++i) vf[i] = trpair(lds + va + i * 1024, lds + va + i * 1024 + 256);
#pragma unroll
    for (int i = 0; i < 7; ++i) {
      ALOAD(wf[WE - 7 + i], vo, rws, sW + (((WE - 7 + i) / 7) ^ rf) * 7168 + (WE - 7 + i) % 7 * 1024);
#pragma unroll
      for (int j = 0; j < 4; ++j) dot(vf[i][j], j & 1, i == 0 && j < 2);
      SCHED();
    }
    sq_put(0);
  }
  __builtin_amdgcn_s_setprio(3);
  SCHED();
  ST(3);

  const uint32_t sD = OFF_WD + (ct * 56 + 7 * w) * 4096;
  const uint32_t sH = OFF_HH + (mt * 4 * 112 + 14 * w) * 1024 + ((ROT & 8 ? rp ^ w : rp) & 1) * 256, vH = g * 512 + l15 * 16;
  constexpr uint32_t HX = ROT & 8 ? 256 : 0;
  const uint32_t sF = OFF_FL + mt * (14 * 128);
  u32x4 wd[28], hh[4][7];
  uint32_t pf, pr;
  f32x16 acc[2];
  float o[8], rs[2], c1[2], gu;
  f32x4 ps;
  u32x4 bf[PB], vf[2];
  auto glu = [&](int p, int h) {
    const int e = h >> 1;
    if (h & 1) {
      if (e >= 1) { o[e - 1] *= gu; TIE(o[e - 1]); }
      if (e < 8) { o[e] = __builtin_amdgcn_rcpf(1.f + o[e]); TIE(o[e]); }
    } else {
      if (e >= 1) { gu = acc[p][7 + e] * rs[p]; TIE(gu); }
      if (e < 8) { o[e] = __builtin_amdgcn_exp2f(acc[p][e] * c1[p]); TIE(o[e]); }
    }
  };
  auto publish = [&](int q) {
    BST((u32x4{umin(pk(o[0], o[1]), SENT - 1), umin(pk(o[2], o[3]), SENT - 1), umin(pk(o[4], o[5]), SENT - 1), umin(pk(o[6], o[7]), SENT - 1)}),
        rws, vo ^ (w & 2 ? HX : 0), sP + q * (112 * 1024), AUX_H);
  };
  auto pollh = [&](int q) { ALOAD1C(pf, min((14 * w >> 3) + (lane & 3), (14 * w + 13) >> 3) * 128, rws, sF + q * 32); };
  auto pollr = [&]() { ALOAD1C(pr, (lane & 1) * 128, rws, sF + (224 + 2 * ct) * 128); };
  auto flagwait = [&](int q) {
    for (int spin = 0; __builtin_amdgcn_ballot_w64(pf == 0) != 0 && spin < (1 << 14); ++spin) {
      __builtin_amdgcn_s_sleep(1);
      pollh(q);
      VMWT(0, pf);
    }
  };
  uint32_t arr;
  uint32_t* const cnt = (uint32_t*)__builtin_assume_aligned(lds + L_CNT, 16) + lane;
  auto flag = [&](int q) {
    __builtin_amdgcn_raw_buffer_store_b32(1u, rws, lane == 0 && arr == 7 ? 0u : 0x80000000u, sF + nt * 128 + q * 32, 16);
  };
  const uint32_t l0 = (uint32_t)(uintptr_t)(LDSP char*)lds;
  auto issue = [&](auto IT) {
    constexpr int it = IT;
    if constexpr (it >= 0 && it < I_DM) ALOAD(wf[it], vo, rws, sW + ((it / 7) ^ rf) * 7168 + it % 7 * 1024);
    if constexpr (it >= I_DM && it < I_WD) ADMA(lb + 2 * UQ + (it - I_DM) * 1024, vo, rws, sU + 2 * UQ + (it - I_DM) * 1024);
    if constexpr (it >= I_WD && it < I_H0) {
      constexpr int k = it - I_WD;
      if constexpr (st_of(k) < 0) ALOAD(wd[k], vo, rws, sD + ((k / 7) ^ rd) * 1024 + k % 7 * 4096);
      else ADMA(l0 + stage(w, st_of(k)), vo, rws, sD + ((k / 7) ^ rd) * 1024 + k % 7 * 4096);
    }
    if constexpr (it >= I_H0 && it < I_END) ALOADC(hh[(it - I_H0) / 7][(it - I_H0) % 7], vH, rws, (sH ^ ((it - I_H0) % 7 & 1 ? HX : 0)) + (it - I_H0) / 7 * (112 * 1024) + (it - I_H0) % 7 * 2048);
    if constexpr (it >= I_PB && it < I_PB + 4) publish(it - I_PB);
    if constexpr (it == I_FLAG || it == I_FLAG + 1) flag(it - I_FLAG);
    if constexpr (it == I_POLL || it == I_POLL + 1) pollh(it - I_POLL);
    if constexpr (it == I_POLLR) pollr();
    if constexpr (it >= I_MK && it < I_MK + 2) BST((u32x4{SENT, SENT, SENT, SENT}), rws, vo, sP + (2 + it - I_MK) * (112 * 1024), AUX_H);
  };
  auto unstage = [&](auto J) { constexpr int j = J; wd[j + 16] = *(const u32x4*)(lds + stage(w, j) + vo); };
#pragma unroll
  for (int i = 0; i < PB - 1; ++i) bf[i] = trpair(lds + bo[0] + i * 1024, lds + bo[0] + i * 1024 + 256);
  sfor<0, 112>([&](auto I) {
    constexpr int i = I, u = i % 56, q = step_q(i), ks = step_k(i);
    if constexpr (i == SB2) { ST(4); VMW(SC.cnt[i] - SC.pos[I_WD - 1] - 1); BAR(); ST(5); }
    if constexpr (i == SB1) BAR();
    if constexpr (i == SB3) { STF(11); BAR(); ST(6); }
    if constexpr (i + PB - 1 < 112) {
      constexpr int n = i + PB - 1, off = (step_q(n) & 1) * UQ + step_k(n) * 1024;
      bf[n % PB] = trpair(lds + bo[n / 56] + off, lds + bo[n / 56] + off + 256);
    }
    constexpr int v = i - V1;
    if constexpr (v >= 0 && v < 28 && v % 4 == 0) vf[(v / 4) & 1] = trpair(lds + va + 2 * UQ + v * 256, lds + va + 2 * UQ + v * 256 + 256);
    if constexpr (q == 0) VMWT(SC.cnt[i] - SC.pos[ks] - 1, wf[ks]);
    if constexpr (i > 0 && prio(i) != prio(i - 1)) __builtin_amdgcn_s_setprio(prio(i));
    if constexpr (SC.at[i] == I_H0 || SC.at[i] == I_H1) {
      constexpr int fq = SC.at[i] == I_H1;
      if constexpr (!fq) { STF(12); }
      TIE(acc[0]); TIE(acc[1]); VMWT(SC.cnt[i] - SC.pos[I_POLL + fq] - 1, pf); flagwait(fq);
      if constexpr (!fq) { STF(13); }
      if constexpr (fq) { ST(7); }
    }
    if constexpr (ks == 0) {
      const f32x16 z = {0.f, 0.f, 0.f, 0.f, 0.f, 0.f, 0.f, 0.f, 0.f, 0.f, 0.f, 0.f, 0.f, 0.f, 0.f, 0.f};
      acc[q & 1] = mfma32(wf[ks], bf[i % PB], z);
    } else {
      acc[q & 1] = mfma32(wf[ks], bf[i % PB], acc[q & 1]);
    }
    if constexpr (v >= 3 && v < 31) { dot(vf[((v - 3) / 4) & 1][(v - 3) % 4], v & 1, v < 5); TIE(sq[v & 1]); }
    if constexpr (v == 31) sq_put(1);
    if constexpr (ks == 25) ps = *(const f32x4*)(lds + L_SMALL + q * 512 + ml * 16);
    if constexpr (ks == 27) { rs[q & 1] = __builtin_amdgcn_rsqf(((ps[0] + ps[1]) + (ps[2] + ps[3])) * (1.f / D) + 1e-5f); TIE(rs[q & 1]); }
    if constexpr (ks == 27) { c1[q & 1] = rs[q & 1] * -1.44269504088896f; }
    if constexpr (i >= G0 && i < G0 + 9) { glu(0, 2 * (i - G0)); glu(0, 2 * (i - G0) + 1); }
    if constexpr (i >= 57 && i < 75) glu(1, i - 57);
    if constexpr (i >= G2 && i < G2 + 18 / GD2) {
#pragma unroll
      for (int h = 0; h < GD2; ++h) glu(0, GD2 * (i - G2) + h);
    }
    if constexpr (i < 110 && (SC.at[i + 2] == I_FLAG || SC.at[i + 2] == I_FLAG + 1)) {
      constexpr int fq = SC.at[i + (i < 110 ? 2 : 0)] - I_FLAG;
      STF(1 + fq); VMW(SC.cnt[i] - SC.pos[I_PB + fq] - 1); STF(9 + fq);
      arr = __hip_atomic_fetch_add(cnt + fq * 64, 1u, __ATOMIC_RELAXED, __HIP_MEMORY_SCOPE_WORKGROUP);
    }
    issue(std::integral_constant<int, SC.at[i]>{});
    if constexpr (SC.cp[i] == 0) VMW(SC.cnt[i + 1] - SC.pos[I_H0 - 2] - 1);
    if constexpr (SC.cp[i] >= 0) unstage(std::integral_constant<int, SC.cp[i]>{});
    SCHED();
  });
  ST(8);
  sfor<SC.ncp, NS>(unstage);
  auto fetch = [&](int q, int i) { ALOADC(hh[q][i], vH, rws, (sH ^ (i & 1 ? HX : 0)) + q * (112 * 1024) + i * 2048); };
  auto check = [&](int q) {
#pragma unroll
    for (int i = 0; i < 7; ++i) TIE(hh[q][i]);
    for (int spin = 0; spin < (1 << 14); ++spin) {
      uint32_t m = 0;
#pragma unroll
      for (int i = 0; i < 7; ++i) m = max3u(max3u(m, hh[q][i][0], hh[q][i][1]), hh[q][i][2], hh[q][i][3]);
      if (__builtin_amdgcn_ballot_w64(m == SENT) == 0) break;
#pragma unroll
      for (int i = 0; i < 7; ++i) ARELOADC(hh[q][i], vH, rws, (sH ^ (i & 1 ? HX : 0)) + q * (112 * 1024) + i * 2048);
      VMW(0);
#pragma unroll
      for (int i = 0; i < 7; ++i) TIE(hh[q][i]);
    }
  };
  f32x4 a2[4][4];
  const f32x4 zero = {0.f, 0.f, 0.f, 0.f};
  auto gemm2 = [&](int q) {
#pragma unroll
    for (int i = 0; i < 7; ++i) {
#pragma unroll
      for (int cf = 0; cf < 4; ++cf) { a2[q][cf] = mfma16(wd[7 * cf + i], hh[q][i], i ? a2[q][cf] : zero); TIE(a2[q][cf]); }
      if (q == F2) fetch(2, i);
      if (q == F3) fetch(3, i);
      SCHED();
    }
  };
  auto spill = [&](int q) {
#pragma unroll
    for (int cf = 0; cf < 4; ++cf) *(f32x4*)(lds + ((q * 8 + w) * 16 + l15) * RED + (cf ^ rd) * 64 + g * 16) = a2[q][cf];
  };
  const int er = 2 * w + (lane >> 5), ec = 64 * ct + 2 * ml;
  uint32_t rc[4];
  const rsrc_t ro = mk(out, 2048 * 896);
  auto reduce = [&](int q) {
    f32x2 s = {lo16(rc[q]), hi16(rc[q])};
#pragma unroll
    for (int ww = 0; ww < 8; ++ww) s += *(const f32x2*)(lds + ((q * 8 + ww) * 16 + er) * RED + ml * 8);
    const int v = (int)pk(s[0], s[1]);
    const u32x4 ov = {(uint32_t)v, (uint32_t)__builtin_amdgcn_update_dpp(0, v, 0x55, 0xf, 0xf, true),
                      (uint32_t)__builtin_amdgcn_update_dpp(0, v, 0xaa, 0xf, 0xf, true),
                      (uint32_t)__builtin_amdgcn_update_dpp(0, v, 0xff, 0xf, 0xf, true)};
    BST(ov, ro, (lane & 3) == 0 ? er * (128 * 896) + ec * 2 : 0x80000000u, (8 * mt + 2 * q + rp) * 896, AUX_L);
  };
  __builtin_amdgcn_s_setprio(3);
#pragma unroll
  for (int i = 0; i < 18; ++i) glu(1, i);
  publish(3);
  __builtin_amdgcn_s_setprio(0);
  STT(9);
  asm volatile("s_waitcnt lgkmcnt(0)" ::: "memory");
  BAR();
  VMW(SC.pos[I_END] - SC.last0);
#pragma unroll
  for (int i = 0; i < 28; ++i)
    if (st_of(i) < 0) TIE(wd[i]);
#pragma unroll
  for (int i = 0; i < 7; ++i) TIE(hh[0][i]);
  gemm2(0);
  spill(0);
  VMWT(SC.pos[I_END] - SC.pos[I_END - 1] + (F2 == 0 ? 7 : 0), pr);
#pragma unroll
  for (int i = 0; i < 7; ++i) TIE(hh[1][i]);
  for (int spin = 0; __builtin_amdgcn_ballot_w64(pr == 0) != 0 && spin < (1 << 14); ++spin) {
    __builtin_amdgcn_s_sleep(1);
    pollr();
    VMWT(0, pr);
  }
  {
    const uint32_t vR = (ml >> 4) * 8192 + ((ml >> 3) & 1) * 1024 + (((ml >> 1) & 1) * 32 + rp * 16 + er) * 16 + ((ml >> 2) & 1) * 8 + (ml & 1) * 4;
#pragma unroll
    for (int q = 0; q < 4; ++q) ALOAD1C(rc[q], vR, rws, OFF_R + (mt * 14 + 2 * ct) * 8192 + q * 2048);
  }
  STT(1);
  gemm2(1);
  STT(2);
  spill(1);
  STT(10);
  SCHED();
  asm volatile("s_waitcnt vmcnt(%4)" : "+v"(rc[0]), "+v"(rc[1]), "+v"(rc[2]), "+v"(rc[3]) : "n"(F3 == 1 && F2 == 0 ? 7 : F3 == 1 ? 1 : 0) : "memory");
  check(2);
  STT(11);
  gemm2(2);
  spill(2);
  asm volatile("s_waitcnt lgkmcnt(0)" ::: "memory");
  BAR();
  STT(12);
  reduce(0); reduce(1); reduce(2);
  SCHED();
  VMW(3);
  check(3);
  STT(13);
  gemm2(3);
  spill(3);
  asm volatile("s_waitcnt lgkmcnt(0)" ::: "memory");
  BAR();
  reduce(3);
  asm volatile("k3_tail_a:");
  ST_END(ws, 3);
}

extern "C" void kernel_launch(void* const* d_in, const int* in_sizes, int n_in, void* d_out, int out_size,
                              void* d_ws, size_t ws_size, hipStream_t stream) {
  const uint16_t* x = (const uint16_t*)d_in[0];
  const uint16_t* wr = (const uint16_t*)d_in[1];
  const uint16_t* wi = (const uint16_t*)d_in[2];
  const uint16_t* wg = (const uint16_t*)d_in[4];
  const uint16_t* wu = (const uint16_t*)d_in[5];
  const uint16_t* wd = (const uint16_t*)d_in[6];
  char* ws = (char*)d_ws;
  k1<<<256, 512, 0, stream>>>(x, wr, wi, wg, wu, wd, ws);
  k3<<<256, 512, 0, stream>>>(ws, (uint16_t*)d_out);
}
```
